```python
import torch

def _softmax(x, axis=-1):
    return torch.softmax(x, dim=axis)
def _gelu(x):
    return torch.nn.functional.gelu(x, approximate="tanh")
def _silu(x):
    return torch.nn.functional.silu(x)
def _sigmoid(x):
    return torch.sigmoid(x)
def _layernorm(x, axis=-1, eps=1e-5):
    m = x.mean(dim=axis, keepdim=True)
    v = x.var(dim=axis, keepdim=True, unbiased=False)
    return (x - m) / torch.sqrt(v + eps)
def _scatter_add(tgt, idx, src, axis=0):
    return tgt.clone().index_add_(axis, idx, src)
def _segment_sum(src, seg, n):
    out = torch.zeros((n,) + src.shape[1:], dtype=src.dtype)
    return out.index_add_(0, seg, src)
def _pad(x, pw):
    flat = tuple(v for pair in reversed(pw) for v in pair)
    return torch.nn.functional.pad(x, flat)

kernel_name = 'standalone__gqa_block_cached__sampling'

def setup_inputs():

    _w = (lambda a: (lambda t: t.to(torch.bfloat16) if t.is_floating_point() else t)(torch.as_tensor(a)))
    B, Sq, D = 8, 1, 448
    Hq, Hkv, Sk, Dh = 16, 8, 8192, 256
    assert Hq % Hkv == 0, (Hq, Hkv)
    return {
        "x": _w((lambda *s: torch.randn(*s, dtype=torch.bfloat16) * 0.8)(B, Sq, D)),
        "k_cache": _w((lambda *s: torch.randn(*s, dtype=torch.bfloat16) * 0.8)(B, Hkv, Sk, Dh)),
        "v_cache": _w((lambda *s: torch.randn(*s, dtype=torch.bfloat16) * 0.05)(B, Hkv, Sk, Dh)),
        "wq": _w((lambda *s: torch.randn(*s, dtype=torch.bfloat16) * 0.8)(D, Hq * Dh) * (D ** -0.5)),
        "wo": _w((lambda *s: torch.randn(*s, dtype=torch.bfloat16) * 0.05)(Hq * Dh, D) * ((Hq * Dh) ** -0.5)),
    }

def reference(x, k_cache, v_cache, wq, wo):
    B, Sq, D = x.shape
    Hq, Dh = 16, 256
    Hkv = k_cache.shape[1]
    rep = Hq // Hkv
    q = (lambda x, *d: x.permute(*d))((x @ wq).reshape(B, Sq, Hq, Dh), 0, 2, 1, 3)
    k = (lambda x, n, axis: torch.repeat_interleave(x, n, dim=axis))(k_cache, rep, axis=1)
    v = (lambda x, n, axis: torch.repeat_interleave(x, n, dim=axis))(v_cache, rep, axis=1)
    scores = torch.einsum("bhqd,bhkd->bhqk", q, k) * (Dh ** -0.5)
    p = _softmax(scores, axis=-1)
    o = torch.einsum("bhqk,bhkd->bhqd", p, v)
    return (lambda x, *d: x.permute(*d))(o, 0, 2, 1, 3).reshape(B, Sq, Hq * Dh) @ wo
```

```cpp
#include <hip/hip_runtime.h>
#include <hip/hip_bf16.h>
#include <stdint.h>

typedef unsigned v4u __attribute__((ext_vector_type(4)));
typedef float f32x4 __attribute__((ext_vector_type(4)));
typedef __bf16 bf16x2 __attribute__((ext_vector_type(2)));

constexpr int B = 8, D = 448, HQ = 16, HKV = 8, SK = 8192, DH = 256;
constexpr int NSPLIT = 8;
constexpr int KPS = SK / NSPLIT;
constexpr int WAVES = 4;
constexpr int KPW = KPS / WAVES;

__device__ __forceinline__ float bf2f(unsigned short v) { return __builtin_bit_cast(float, (unsigned)v << 16); }
__device__ __forceinline__ unsigned short f2bf(float f) { __hip_bfloat16 h = __float2bfloat16(f); return __builtin_bit_cast(unsigned short, h); }
__device__ __forceinline__ float dot2(unsigned k, unsigned q, float acc) {
  return __builtin_amdgcn_fdot2_f32_bf16(__builtin_bit_cast(bf16x2, k), __builtin_bit_cast(bf16x2, q), acc, false);
}

__global__ void __launch_bounds__(256) k_qproj(const unsigned short* __restrict__ x, const unsigned short* __restrict__ wq, unsigned short* __restrict__ q) {
  __shared__ float xs[B][D];
  __shared__ float red[4][B][64];
  for (int i = threadIdx.x; i < B * D; i += 256) xs[i / D][i % D] = bf2f(x[i]);
  __syncthreads();
  const int lane = threadIdx.x & 63, w = threadIdx.x >> 6;
  const int n = blockIdx.x * 64 + lane;
  float acc[B];
  for (int b = 0; b < B; ++b) acc[b] = 0.f;
  for (int k = w * 112; k < (w + 1) * 112; ++k) {
    float wv = bf2f(wq[(size_t)k * 4096 + n]);
    for (int b = 0; b < B; ++b) acc[b] = fmaf(xs[b][k], wv, acc[b]);
  }
  for (int b = 0; b < B; ++b) red[w][b][lane] = acc[b];
  __syncthreads();
  for (int i = threadIdx.x; i < B * 64; i += 256) {
    int b = i / 64, l = i % 64;
    float s = red[0][b][l] + red[1][b][l] + red[2][b][l] + red[3][b][l];
    q[b * 4096 + blockIdx.x * 64 + l] = f2bf(s);
  }
}

__global__ void __launch_bounds__(256) k_attn(const unsigned short* __restrict__ q, const unsigned short* __restrict__ kc, const unsigned short* __restrict__ vc,
                                              float* __restrict__ pml, float* __restrict__ po) {
  __shared__ float ps[WAVES][KPW][2];
  __shared__ float mrg[WAVES][2][DH + 4];
  const int s = blockIdx.x % NSPLIT, bh = blockIdx.x / NSPLIT;
  const int b = bh / HKV, kvh = bh % HKV;
  const int lane = threadIdx.x & 63, w = __builtin_amdgcn_readfirstlane(threadIdx.x >> 6);
  const int half = lane >> 5, c = lane & 31;
  v4u q0 = *(const v4u*)(q + b * 4096 + (kvh * 2 + 0) * DH + 8 * c);
  v4u q1 = *(const v4u*)(q + b * 4096 + (kvh * 2 + 1) * DH + 8 * c);
  const size_t base = ((size_t)bh * SK + (size_t)s * KPS + (size_t)w * KPW) * DH;
  const v4u* kp = (const v4u*)(kc + base) + lane;
  const v4u* vp = (const v4u*)(vc + base) + lane;
  float m0 = -INFINITY, m1 = -INFINITY;
  constexpr float C2 = 0.0625f * 1.4426950408889634f;
  for (int i = 0; i < KPW / 2; i += 8) {
    v4u kk[8];
#pragma unroll
    for (int u = 0; u < 8; ++u) kk[u] = __builtin_nontemporal_load(kp + (i + u) * 64);
#pragma unroll
    for (int u = 0; u < 8; ++u) {
      float a0 = 0.f, a1 = 0.f;
#pragma unroll
      for (int j = 0; j < 4; ++j) { a0 = dot2(kk[u][j], q0[j], a0); a1 = dot2(kk[u][j], q1[j], a1); }
#pragma unroll
      for (int o = 16; o >= 1; o >>= 1) { a0 += __shfl_xor(a0, o); a1 += __shfl_xor(a1, o); }
      a0 *= C2; a1 *= C2;
      m0 = fmaxf(m0, a0); m1 = fmaxf(m1, a1);
      if (c == 0) { ps[w][(i + u) * 2 + half][0] = a0; ps[w][(i + u) * 2 + half][1] = a1; }
    }
  }
  m0 = fmaxf(m0, __shfl_xor(m0, 32)); m1 = fmaxf(m1, __shfl_xor(m1, 32));
  __builtin_amdgcn_fence(__ATOMIC_SEQ_CST, "wavefront");
  float l0 = 0.f, l1 = 0.f;
  for (int i = lane; i < KPW; i += 64) {
    float p0 = __builtin_amdgcn_exp2f(ps[w][i][0] - m0), p1 = __builtin_amdgcn_exp2f(ps[w][i][1] - m1);
    ps[w][i][0] = p0; ps[w][i][1] = p1; l0 += p0; l1 += p1;
  }
#pragma unroll
  for (int o = 32; o >= 1; o >>= 1) { l0 += __shfl_xor(l0, o); l1 += __shfl_xor(l1, o); }
  __builtin_amdgcn_fence(__ATOMIC_SEQ_CST, "wavefront");
  float acc0[8], acc1[8];
#pragma unroll
  for (int j = 0; j < 8; ++j) { acc0[j] = 0.f; acc1[j] = 0.f; }
  for (int i = 0; i < KPW / 2; i += 8) {
    v4u vv[8];
#pragma unroll
    for (int u = 0; u < 8; ++u) vv[u] = __builtin_nontemporal_load(vp + (i + u) * 64);
#pragma unroll
    for (int u = 0; u < 8; ++u) {
      float2 p = *(const float2*)&ps[w][(i + u) * 2 + half][0];
#pragma unroll
      for (int j = 0; j < 4; ++j) {
        float lo = __builtin_bit_cast(float, vv[u][j] << 16), hi = __builtin_bit_cast(float, vv[u][j] & 0xffff0000u);
        acc0[2 * j] = fmaf(p.x, lo, acc0[2 * j]); acc0[2 * j + 1] = fmaf(p.x, hi, acc0[2 * j + 1]);
        acc1[2 * j] = fmaf(p.y, lo, acc1[2 * j]); acc1[2 * j + 1] = fmaf(p.y, hi, acc1[2 * j + 1]);
      }
    }
  }
#pragma unroll
  for (int j = 0; j < 8; ++j) { acc0[j] += __shfl_xor(acc0[j], 32); acc1[j] += __shfl_xor(acc1[j], 32); }
  if (half == 0) {
#pragma unroll
    for (int j = 0; j < 8; ++j) { mrg[w][0][4 + 8 * c + j] = acc0[j]; mrg[w][1][4 + 8 * c + j] = acc1[j]; }
    if (c == 0) { mrg[w][0][0] = m0; mrg[w][0][1] = l0; mrg[w][1][0] = m1; mrg[w][1][1] = l1; }
  }
  __syncthreads();
  for (int h = 0; h < 2; ++h) {
    float M = -INFINITY;
    for (int ww = 0; ww < WAVES; ++ww) M = fmaxf(M, mrg[ww][h][0]);
    float L = 0.f, o = 0.f;
    for (int ww = 0; ww < WAVES; ++ww) { float wt = __builtin_amdgcn_exp2f(mrg[ww][h][0] - M); L = fmaf(wt, mrg[ww][h][1], L); o = fmaf(wt, mrg[ww][h][4 + threadIdx.x], o); }
    const size_t slot = ((size_t)(b * HQ + kvh * 2 + h)) * NSPLIT + s;
    po[slot * DH + threadIdx.x] = o;
    if (threadIdx.x == 0) { pml[slot * 2] = M; pml[slot * 2 + 1] = L; }
  }
}

__global__ void __launch_bounds__(256) k_out(const float* __restrict__ pml, const float* __restrict__ po, const unsigned short* __restrict__ wo, unsigned short* __restrict__ out) {
  __shared__ float os[HQ * DH];
  __shared__ float red[4][64];
  const int b = blockIdx.x, cb = blockIdx.y;
  for (int h = 0; h < HQ; ++h) {
    const size_t slot0 = ((size_t)(b * HQ + h)) * NSPLIT;
    float M = -INFINITY;
    for (int s = 0; s < NSPLIT; ++s) M = fmaxf(M, pml[(slot0 + s) * 2]);
    float L = 0.f, o = 0.f;
    for (int s = 0; s < NSPLIT; ++s) { float wt = __builtin_amdgcn_exp2f(pml[(slot0 + s) * 2] - M); L = fmaf(wt, pml[(slot0 + s) * 2 + 1], L); o = fmaf(wt, po[(slot0 + s) * DH + threadIdx.x], o); }
    os[h * DH + threadIdx.x] = bf2f(f2bf(o / L));
  }
  __syncthreads();
  const int lane = threadIdx.x & 63, w = threadIdx.x >> 6;
  const int n = cb * 64 + lane;
  float acc = 0.f;
  for (int k = w * 1024; k < (w + 1) * 1024; ++k) acc = fmaf(os[k], bf2f(wo[(size_t)k * D + n]), acc);
  red[w][lane] = acc;
  __syncthreads();
  if (threadIdx.x < 64) out[b * D + n] = f2bf(red[0][lane] + red[1][lane] + red[2][lane] + red[3][lane]);
}

extern "C" void kernel_launch(void* const* d_in, const int* in_sizes, int n_in, void* d_out, int out_size, void* d_ws, size_t ws_size, hipStream_t stream) {
  const unsigned short* x = (const unsigned short*)d_in[0];
  const unsigned short* kc = (const unsigned short*)d_in[1];
  const unsigned short* vc = (const unsigned short*)d_in[2];
  const unsigned short* wq = (const unsigned short*)d_in[3];
  const unsigned short* wo = (const unsigned short*)d_in[4];
  unsigned char* ws = (unsigned char*)d_ws;
  unsigned short* q = (unsigned short*)ws;
  float* pml = (float*)(ws + 65536);
  float* po = (float*)(ws + 65536 + 65536);
  k_qproj<<<64, 256, 0, stream>>>(x, wq, q);
  k_attn<<<B * HKV * NSPLIT, 256, 0, stream>>>(q, kc, vc, pml, po);
  k_out<<<dim3(B, 7), 256, 0, stream>>>(pml, po, wo, (unsigned short*)d_out);
}
```

```cpp
#include <hip/hip_runtime.h>
#include <hip/hip_bf16.h>
#include <stdint.h>

typedef unsigned v4u __attribute__((ext_vector_type(4)));
typedef unsigned v2u __attribute__((ext_vector_type(2)));
typedef float f32x4 __attribute__((ext_vector_type(4)));
typedef float f32x2 __attribute__((ext_vector_type(2)));
typedef short s16x4 __attribute__((ext_vector_type(4)));
typedef __bf16 bf16x8 __attribute__((ext_vector_type(8)));
typedef __bf16 bf16x2 __attribute__((ext_vector_type(2)));
typedef unsigned long long u64;
#define LAS __attribute__((address_space(3)))
#define GAS __attribute__((address_space(1)))
#define RLX_AGENT __ATOMIC_RELAXED, __HIP_MEMORY_SCOPE_AGENT

constexpr int B = 8, D = 448, HQ = 16, HKV = 8, SK = 8192, DH = 256;
constexpr int NSPLIT = 4;
constexpr int KPS = SK / NSPLIT;
#define INFL 2
#define WO_NT 0
#define WOMODE 0
#define WQ_AUX 2
constexpr int TILE_B = 16384, NSLOT = 8, NKT = KPS / 32, NT = 2 * NKT;
constexpr int NCONS = 4;
constexpr int SPIN_CAP = 1 << 14;
constexpr int GSPIN_CAP = 1 << 13;
constexpr int S_OFF = NSLOT * TILE_B;
constexpr int P_OFF = S_OFF + 2 * KPS * 4;
constexpr int Q_OFF = P_OFF + NKT * 128;
constexpr int R_OFF = Q_OFF + 1024;
constexpr int C_OFF = R_OFF + 256;
constexpr int LDS_BYTES = C_OFF + 256;
constexpr int WS_E = 0;
constexpr int WS_QG = 4096;
constexpr int WS_ST = WS_QG + 8 * 2048 * 8;
constexpr int WS_PO = WS_ST + 8 * 16 * 4 * 16;
constexpr int WS_BYTES = WS_PO + 8 * 16 * 4 * 256 * 4;

__device__ __forceinline__ unsigned cvtpk(float lo, float hi) { f32x2 v = {lo, hi}; return __builtin_bit_cast(unsigned, __builtin_convertvector(v, bf16x2)); }
__device__ __forceinline__ f32x4 mfma(v4u a, v4u b, f32x4 c) {
  return __builtin_amdgcn_mfma_f32_16x16x32_bf16(__builtin_bit_cast(bf16x8, a), __builtin_bit_cast(bf16x8, b), c, 0, 0, 0);
}
__device__ __forceinline__ float dot2(unsigned k, unsigned q, float acc) {
  return __builtin_amdgcn_fdot2_f32_bf16(__builtin_bit_cast(bf16x2, k), __builtin_bit_cast(bf16x2, q), acc, false);
}
__device__ __forceinline__ v2u trread(LAS unsigned char* p) { return __builtin_bit_cast(v2u, __builtin_amdgcn_ds_read_tr16_b64_v4i16((LAS s16x4*)p)); }
__device__ __forceinline__ float x16max(float x) {
  auto s = __builtin_amdgcn_permlane16_swap(__float_as_uint(x), __float_as_uint(x), false, false);
  x = fmaxf(__uint_as_float(s[0]), __uint_as_float(s[1]));
  auto t = __builtin_amdgcn_permlane32_swap(__float_as_uint(x), __float_as_uint(x), false, false);
  return fmaxf(__uint_as_float(t[0]), __uint_as_float(t[1]));
}
template <int CTRL> __device__ __forceinline__ float dpp(float x) {
  return __builtin_bit_cast(float, __builtin_amdgcn_mov_dpp(__builtin_bit_cast(int, x), CTRL, 0xf, 0xf, true));
}
constexpr int XOR1 = 0xB1, XOR2 = 0x4E, XOR7 = 0x141, XOR8 = 0x128;
__device__ __forceinline__ float swap32add(float a, float b) {
  auto t = __builtin_amdgcn_permlane32_swap(__float_as_uint(a), __float_as_uint(b), false, false);
  return __uint_as_float(t[0]) + __uint_as_float(t[1]);
}
__device__ __forceinline__ float swap16add(float a, float b) {
  auto t = __builtin_amdgcn_permlane16_swap(__float_as_uint(a), __float_as_uint(b), false, false);
  return __uint_as_float(t[0]) + __uint_as_float(t[1]);
}
__device__ __forceinline__ float wavesum(float x) {
  x += dpp<XOR1>(x); x += dpp<XOR2>(x); x += dpp<XOR7>(x); x += dpp<XOR8>(x);
  x = swap16add(x, x); return swap32add(x, x);
}
#define LDSW() asm volatile("s_waitcnt lgkmcnt(0)" ::: "memory")
#define DRAIN() asm volatile("s_waitcnt vmcnt(0)" ::: "memory")
#define RFL(x) __builtin_amdgcn_readfirstlane(x)

__global__ void __launch_bounds__((1 + NCONS) * 64, 1) k_fused(const unsigned short* __restrict__ x, const unsigned short* __restrict__ kc, const unsigned short* __restrict__ vc,
                                              const unsigned short* __restrict__ wq, const unsigned short* __restrict__ wo, unsigned short* __restrict__ out, unsigned char* ws) {
  extern __shared__ __attribute__((aligned(16))) unsigned char lds[];
  LAS unsigned char* L = (LAS unsigned char*)lds;
  volatile LAS unsigned* ctl = (volatile LAS unsigned*)(L + C_OFF);
  const int bid = blockIdx.x;
  const int s = bid & 3, kvh = (bid >> 2) & 7, b = bid >> 5;
  const int lane = threadIdx.x & 63, wave = RFL(threadIdx.x >> 6);
#define STAMP(i)
  if (wave == 0) {
    const unsigned char* kb = (const unsigned char*)kc + (size_t)bid * (KPS * DH * 2);
    const unsigned char* vb = (const unsigned char*)vc + (size_t)bid * (KPS * DH * 2);
    const unsigned hi = lane >> 5, c = lane & 31;
    const unsigned koff = hi * 512 + ((c ^ hi) << 4);
    const unsigned voff = hi * 512 + ((c ^ (hi << 1)) << 4);
    auto issue = [&](int t) {
      const int slot = t & (NSLOT - 1);
      if (t < NKT) {
        const unsigned char* src = kb + (size_t)t * TILE_B;
#pragma unroll
        for (int m = 0; m < 16; ++m)
          __builtin_amdgcn_global_load_lds((const GAS unsigned*)(src + m * 1024 + (koff ^ (((2 * m) & 15) << 4))), (LAS unsigned*)(L + slot * TILE_B + m * 1024), 16, 0, 2);
      } else {
        const unsigned char* src = vb + (size_t)(t - NKT) * TILE_B;
#pragma unroll
        for (int m = 0; m < 16; ++m)
          __builtin_amdgcn_global_load_lds((const GAS unsigned*)(src + m * 1024 + (voff ^ (((4 * m) & 15) << 4))), (LAS unsigned*)(L + slot * TILE_B + m * 1024), 16, 0, 2);
      }
    };
    ((LAS unsigned*)(L + C_OFF))[lane] = 0u;
#pragma unroll
    for (int t = 0; t < INFL; ++t) issue(t);
    LDSW();
    __builtin_amdgcn_s_barrier();
    for (int t = INFL; t < NT; ++t) {
      if (t >= NSLOT) {
        const unsigned needt = (unsigned)(t - NSLOT);
        _Pragma("nounroll") for (int sp = 0; sp < SPIN_CAP * 16; ++sp) {
          const v4u nd = *(volatile LAS v4u*)(L + C_OFF + 16);
          const unsigned mn = min(min(nd.x, nd.y), min(nd.z, nd.w));
          if (RFL(mn) > needt) break;
          __builtin_amdgcn_s_sleep(1);
        }
        asm volatile("" ::: "memory");
      }
      issue(t);
      asm volatile("s_waitcnt vmcnt(%0)" :: "i"(16 * INFL) : "memory");
      ctl[0] = (unsigned)(t - INFL + 1);
    }
#pragma unroll
    for (int r = INFL - 1; r >= 0; --r) {
      asm volatile("s_waitcnt vmcnt(%0)" :: "i"(16 * r) : "memory");
      ctl[0] = (unsigned)(NT - r);
    }
    return;
  }
  const int w = wave - 1;
  const int g = lane >> 4, r16 = lane & 15, head = lane & 1;
  const int qq = (lane >> 2) & 3, pp = lane & 3;
  const __amdgpu_buffer_rsrc_t rs = __builtin_amdgcn_make_buffer_rsrc((void*)ws, 0, WS_BYTES, 0x00020000);
  unsigned eraw = __hip_atomic_load((GAS unsigned*)(ws + WS_E), RLX_AGENT);
  unsigned tag;
  if (w == 0) {
    const int ctile = (bid & 7) * 32 + (bid >> 3);
    const unsigned short* wsrc = wq + (size_t)(lane >> 1) * 4096 + ctile * 16 + (lane & 1) * 8;
#pragma unroll
    for (int j = 0; j < 14; ++j) __builtin_amdgcn_global_load_lds((const GAS unsigned*)(wsrc + (size_t)j * 32 * 4096), (LAS unsigned*)(L + S_OFF + j * 1024), 16, 0, WQ_AUX);
    v4u xf[14];
#pragma unroll
    for (int j = 0; j < 14; ++j) xf[j] = *(const GAS v4u*)(x + (lane & 7) * D + 32 * j + 8 * g);
    __builtin_amdgcn_sched_barrier(0);
    __builtin_amdgcn_s_barrier();
    DRAIN();
    asm volatile("" : "+v"(eraw));
    tag = RFL(eraw) + 1u;
    f32x4 d = {0.f, 0.f, 0.f, 0.f}, d2 = {0.f, 0.f, 0.f, 0.f};
    LAS unsigned char* wb = L + S_OFF + (8 * g + qq) * 32 + pp * 8;
    v4u wf[14];
#pragma unroll
    for (int j = 0; j < 14; ++j) { const v2u lo = trread(wb + j * 1024), hi = trread(wb + j * 1024 + 128); wf[j] = v4u{lo.x, lo.y, hi.x, hi.y}; }
    __builtin_amdgcn_sched_barrier(0);
#pragma unroll
    for (int j = 0; j < 14; j += 2) { d = mfma(wf[j], xf[j], d); d2 = mfma(wf[j + 1], xf[j + 1], d2); }
    d += d2;
    if (r16 < 8) {
      const v4u gr = {cvtpk(d[0], d[1]), tag, cvtpk(d[2], d[3]), tag};
      __builtin_amdgcn_raw_buffer_store_b128(gr, rs, WS_QG + (r16 * 2048 + ctile * 8 + 2 * g) * 8, 0, 16);
    }
    STAMP(3);
    const int qo = WS_QG + (b * 2048 + kvh * 256) * 8 + lane * 16;
    v4u ga, gb;
    _Pragma("nounroll") for (int sp = 0; sp < GSPIN_CAP; ++sp) {
      ga = __builtin_amdgcn_raw_buffer_load_b128(rs, qo, 0, 16);
      gb = __builtin_amdgcn_raw_buffer_load_b128(rs, qo + 1024, 0, 16);
      const bool ok = ga.y == tag && ga.w == tag && gb.y == tag && gb.w == tag;
      if (__all(ok)) break;
      __builtin_amdgcn_s_sleep(2);
    }
    *(LAS v2u*)(L + Q_OFF + lane * 8) = v2u{ga.x, ga.z};
    *(LAS v2u*)(L + Q_OFF + 512 + lane * 8) = v2u{gb.x, gb.z};
    LDSW();
    ctl[10] = 1u;
    STAMP(4);
  } else {
    if (w == 1) {
      const v4u sent = {~0u, ~0u, ~0u, ~0u};
      __builtin_amdgcn_raw_buffer_store_b128(sent, rs, WS_PO + ((b * 16 + kvh * 2) * 4 + s) * 1024 + lane * 16, 0, 16);
      __builtin_amdgcn_raw_buffer_store_b128(sent, rs, WS_PO + ((b * 16 + kvh * 2 + 1) * 4 + s) * 1024 + lane * 16, 0, 16);
    }
    __builtin_amdgcn_s_barrier();
    _Pragma("nounroll") for (int sp = 0; sp < SPIN_CAP * 16 && RFL(ctl[10]) == 0u; ++sp) { __builtin_amdgcn_s_sleep(2); }
    asm volatile("" : "+v"(eraw) :: "memory");
    tag = RFL(eraw) + 1u;
  }
  v4u qf[8];
#pragma unroll
  for (int i = 0; i < 8; ++i) qf[i] = *(const LAS v4u*)(L + Q_OFF + head * 512 + 64 * i + 16 * g);
  auto wait_full = [&](unsigned n) {
    _Pragma("nounroll") for (int sp = 0; sp < SPIN_CAP && RFL(ctl[0]) < n; ++sp) { __builtin_amdgcn_s_sleep(1); }
    asm volatile("" ::: "memory");
  };
  float mx = -INFINITY;
  {
    unsigned ka[4];
#pragma unroll
    for (int i = 0; i < 4; ++i) ka[i] = r16 * 512 + (((4 * i + g) ^ r16) << 4);
    for (int t = w; t < NKT; t += NCONS) {
      wait_full(t + 1);
      LAS unsigned char* tb = L + (t & (NSLOT - 1)) * TILE_B;
      v4u a0[8], a1[8];
#pragma unroll
      for (int i = 0; i < 8; ++i) { a0[i] = *(const LAS v4u*)(tb + ka[i & 3] + (i >> 2) * 256); a1[i] = *(const LAS v4u*)(tb + ka[i & 3] + (i >> 2) * 256 + 8192); }
      LDSW();
      ctl[4 + w] = (unsigned)(t + NCONS < NKT ? t + NCONS : NKT);
      asm volatile("" : "+v"(a0[0]), "+v"(a0[1]), "+v"(a0[2]), "+v"(a0[3]), "+v"(a0[4]), "+v"(a0[5]), "+v"(a0[6]), "+v"(a0[7]),
                        "+v"(a1[0]), "+v"(a1[1]), "+v"(a1[2]), "+v"(a1[3]), "+v"(a1[4]), "+v"(a1[5]), "+v"(a1[6]), "+v"(a1[7]) :: "memory");
      f32x4 d0 = {0.f, 0.f, 0.f, 0.f}, d1 = {0.f, 0.f, 0.f, 0.f};
#pragma unroll
      for (int i = 0; i < 8; ++i) { d0 = mfma(a0[i], qf[i], d0); d1 = mfma(a1[i], qf[i], d1); }
      mx = fmaxf(mx, fmaxf(fmaxf(fmaxf(d0[0], d0[1]), fmaxf(d0[2], d0[3])), fmaxf(fmaxf(d1[0], d1[1]), fmaxf(d1[2], d1[3]))));
      if (r16 < 4) {
        const f32x4 d = (r16 & 2) ? d1 : d0;
        *(LAS f32x4*)(L + S_OFF + head * (KPS * 4) + (t * 32 + (r16 & 2) * 8 + 4 * g) * 4) = d;
      }
    }
  }
  mx = x16max(mx);
  if (lane < 2) ((LAS float*)(L + C_OFF))[16 + w * 2 + lane] = mx;
  LDSW();
  if (lane == 0) __hip_atomic_fetch_add((LAS unsigned*)(L + C_OFF) + 8, 1u, __ATOMIC_RELAXED, __HIP_MEMORY_SCOPE_WORKGROUP);
  _Pragma("nounroll") for (int sp = 0; sp < SPIN_CAP && RFL(ctl[8]) < NCONS; ++sp) { __builtin_amdgcn_s_sleep(1); }
  asm volatile("" ::: "memory");
  constexpr float C2 = 0.0625f * 1.4426950408889634f;
  {
    const f32x4 ma = *(const LAS f32x4*)(L + C_OFF + 64), mb = *(const LAS f32x4*)(L + C_OFF + 80);
    const float M0 = fmaxf(fmaxf(ma[0], ma[2]), fmaxf(mb[0], mb[2])) * C2, M1 = fmaxf(fmaxf(ma[1], ma[3]), fmaxf(mb[1], mb[3])) * C2;
    const int tt = w * 64 + lane, tile = tt >> 2, gg = tt & 3;
    float sum[2];
#pragma unroll
    for (int h = 0; h < 2; ++h) {
      const f32x4 sa = *(const LAS f32x4*)(L + S_OFF + h * (KPS * 4) + (tile * 32 + 4 * gg) * 4);
      const f32x4 sb = *(const LAS f32x4*)(L + S_OFF + h * (KPS * 4) + (tile * 32 + 16 + 4 * gg) * 4);
      const float mc = h ? M1 : M0;
      float p[8];
#pragma unroll
      for (int j = 0; j < 4; ++j) { p[j] = __builtin_amdgcn_exp2f(fmaf(sa[j], C2, -mc)); p[4 + j] = __builtin_amdgcn_exp2f(fmaf(sb[j], C2, -mc)); }
      sum[h] = ((p[0] + p[1]) + (p[2] + p[3])) + ((p[4] + p[5]) + (p[6] + p[7]));
      v4u pk = {cvtpk(p[0], p[1]), cvtpk(p[2], p[3]), cvtpk(p[4], p[5]), cvtpk(p[6], p[7])};
      *(LAS v4u*)(L + P_OFF + tile * 128 + gg * 32 + h * 16) = pk;
    }
    sum[0] = wavesum(sum[0]); sum[1] = wavesum(sum[1]);
    if (lane < 2) ((LAS float*)(L + C_OFF))[24 + w * 2 + lane] = lane ? sum[1] : sum[0];
    LDSW();
    if (lane == 0) __hip_atomic_fetch_add((LAS unsigned*)(L + C_OFF) + 9, 1u, __ATOMIC_RELAXED, __HIP_MEMORY_SCOPE_WORKGROUP);
    _Pragma("nounroll") for (int sp = 0; sp < SPIN_CAP && RFL(ctl[9]) < NCONS; ++sp) { __builtin_amdgcn_s_sleep(1); }
    asm volatile("" ::: "memory");
    if (w == 1) {
      DRAIN();
      if (lane < 2) {
        const f32x4 la = *(const LAS f32x4*)(L + C_OFF + 96), lb = *(const LAS f32x4*)(L + C_OFF + 112);
        const float Ls = lane ? ((la[1] + la[3]) + (lb[1] + lb[3])) : ((la[0] + la[2]) + (lb[0] + lb[2]));
        const v4u gr = {__float_as_uint(lane ? M1 : M0), tag, __float_as_uint(Ls), tag};
        __builtin_amdgcn_raw_buffer_store_b128(gr, rs, WS_ST + ((b * 16 + kvh * 2 + lane) * 4 + s) * 16, 0, 16);
      }
    }
  }
  const bool has_out = (bid & 7) < 7;
  const int ct = has_out ? (bid & 7) * 4 + ((bid >> 3) & 3) : 0;
  f32x4 acc[4];
#pragma unroll
  for (int u = 0; u < 4; ++u) acc[u] = f32x4{0.f, 0.f, 0.f, 0.f};
  v4u wv[32];
  float wt[16];
  {
    const int key = 4 * g + qq;
    unsigned va[4];
#pragma unroll
    for (int u = 0; u < 4; ++u) va[u] = key * 512 + (((8 * w + 2 * u + (pp >> 1)) ^ (2 * (key & 7))) << 4) + (pp & 1) * 8;
    const unsigned pa = P_OFF + g * 32 + head * 16;
    auto vtile = [&](int t) {
      wait_full(NKT + t + 1);
      LAS unsigned char* tb = L + ((NKT + t) & (NSLOT - 1)) * TILE_B;
      v4u pf = *(const LAS v4u*)(L + pa + t * 128);
      v4u av[4];
#pragma unroll
      for (int u = 0; u < 4; ++u) { const v2u lo = trread(tb + va[u]), hi = trread(tb + va[u] + 8192); av[u] = v4u{lo.x, lo.y, hi.x, hi.y}; }
      LDSW();
      ctl[4 + w] = (unsigned)(NKT + t + 1);
      asm volatile("" : "+v"(av[0]), "+v"(av[1]), "+v"(av[2]), "+v"(av[3]), "+v"(pf) :: "memory");
#pragma unroll
      for (int u = 0; u < 4; ++u) acc[u] = mfma(av[u], pf, acc[u]);
    };
    const unsigned short* wop = wo + (size_t)(w * 256 + 4 * lane) * D + ct * 16;
#pragma unroll
    for (int t = 0; t < 32; ++t) {
      const GAS v4u* p = (const GAS v4u*)(wop + (size_t)((t >> 3) * 1024 + ((t >> 1) & 3)) * D + (t & 1) * 8);
      wv[t] = WO_NT ? __builtin_nontemporal_load(p) : *p;
      __builtin_amdgcn_sched_barrier(0);
      vtile(t);
      __builtin_amdgcn_sched_barrier(0);
    }
#pragma nounroll
    for (int t = 32; t < 48; ++t) vtile(t);
    {
      v4u st;
      const int so = WS_ST + ((b * 16 + w + 4 * ((lane >> 2) & 3)) * 4 + (lane & 3)) * 16;
      _Pragma("nounroll") for (int sp = 0; sp < GSPIN_CAP; ++sp) {
        st = __builtin_amdgcn_raw_buffer_load_b128(rs, so, 0, 16);
        if (__all(st.y == tag && st.w == tag)) break;
        __builtin_amdgcn_s_sleep(8);
      }
      const float ms = __uint_as_float(st.x), ls = __uint_as_float(st.z);
      float M = fmaxf(ms, dpp<XOR1>(ms)); M = fmaxf(M, dpp<XOR2>(M));
      const float e = __builtin_amdgcn_exp2f(ms - M);
      float Lt = e * ls; Lt += dpp<XOR1>(Lt); Lt += dpp<XOR2>(Lt);
      const float wgt = e / Lt;
#pragma unroll
      for (int i = 0; i < 16; ++i) wt[i] = __builtin_bit_cast(float, __builtin_amdgcn_readlane(__builtin_bit_cast(int, wgt), i));
#pragma unroll
      for (int m = 0; m < 4; ++m)
#pragma unroll
        for (int j = 0; j < 2; ++j)
#pragma unroll
          for (int hf = 0; hf < 2; ++hf) {
            const v4u ra = wv[(m * 4 + 2 * j) * 2 + hf], rb = wv[(m * 4 + 2 * j + 1) * 2 + hf];
            v4u ev, od;
#pragma unroll
            for (int c = 0; c < 4; ++c) { ev[c] = __builtin_amdgcn_perm(rb[c], ra[c], 0x05040100u); od[c] = __builtin_amdgcn_perm(rb[c], ra[c], 0x07060302u); }
            asm volatile("" : "+v"(ev), "+v"(od));
            wv[(m * 4 + 2 * j) * 2 + hf] = ev; wv[(m * 4 + 2 * j + 1) * 2 + hf] = od;
          }
      __builtin_amdgcn_sched_barrier(0);
    }
#pragma nounroll
    for (int t = 48; t < NKT; ++t) vtile(t);
  }
  STAMP(5 + w);
  {
    const int u = (r16 >> 1) & 3;
    const f32x4 a01 = (u & 1) ? acc[1] : acc[0], a23 = (u & 1) ? acc[3] : acc[2];
    const f32x4 a = (u & 2) ? a23 : a01;
    v4u o = __builtin_bit_cast(v4u, a);
#pragma unroll
    for (int c = 0; c < 4; ++c) o[c] = min(o[c], 0xFFFFFFFEu);
    if (r16 < 8) __builtin_amdgcn_raw_buffer_store_b128(o, rs, WS_PO + ((b * 16 + kvh * 2 + head) * 4 + s) * 1024 + (64 * w + 16 * u + 4 * g) * 4, 0, 16);
  }
  if (!has_out) return;
  v4u pv[16];
  u64 pm[16];
  const int po = WS_PO + (b * 16 + w) * 4096 + lane * 16;
  typedef int v4i __attribute__((ext_vector_type(4)));
  const u64 wsa = (u64)ws;
  const v4i srd = {(int)(unsigned)wsa, (int)((unsigned)(wsa >> 32) & 0xffffu), WS_BYTES, 0x00020000};
#pragma unroll
  for (int m = 0; m < 4; ++m) {
    asm volatile("buffer_load_dwordx4 %0, %4, %5, 0 offen sc1\n\t"
                 "buffer_load_dwordx4 %1, %4, %5, 0 offen offset:1024 sc1\n\t"
                 "buffer_load_dwordx4 %2, %4, %5, 0 offen offset:2048 sc1\n\t"
                 "buffer_load_dwordx4 %3, %4, %5, 0 offen offset:3072 sc1"
                 : "=&v"(pv[4 * m]), "=&v"(pv[4 * m + 1]), "=&v"(pv[4 * m + 2]), "=&v"(pv[4 * m + 3])
                 : "v"(po + m * 16384), "s"(srd) : "memory");
  }
  int lastn = 16;
  _Pragma("nounroll") for (int sp = 0; sp < GSPIN_CAP; ++sp) {
    asm volatile("s_waitcnt vmcnt(0)" : "+v"(pv[0]), "+v"(pv[1]), "+v"(pv[2]), "+v"(pv[3]), "+v"(pv[4]), "+v"(pv[5]), "+v"(pv[6]), "+v"(pv[7]),
                 "+v"(pv[8]), "+v"(pv[9]), "+v"(pv[10]), "+v"(pv[11]), "+v"(pv[12]), "+v"(pv[13]), "+v"(pv[14]), "+v"(pv[15]) :: "memory");
    u64 any = 0;
#pragma unroll
    for (int i = 0; i < 16; ++i) {
      const unsigned mxv = max(max(pv[i].x, pv[i].y), max(pv[i].z, pv[i].w));
      pm[i] = __builtin_amdgcn_ballot_w64(mxv == ~0u);
      any |= pm[i];
    }
    if (!any) break;
    int npend = 0;
#pragma unroll
    for (int i = 0; i < 16; ++i) npend += pm[i] ? 1 : 0;
    const bool thin = npend > 4 && npend == lastn;
    lastn = npend;
    if (thin) {
      bool seen = false;
#pragma unroll
      for (int i = 0; i < 16; ++i) { const u64 t = pm[i]; pm[i] = seen ? 0ull : t; seen = seen || t; }
      __builtin_amdgcn_s_sleep(8);
    }
#pragma unroll
    for (int m = 0; m < 4; ++m) {
      asm volatile("s_mov_b64 exec, %5\n\tbuffer_load_dwordx4 %0, %4, %9, 0 offen sc1\n\t"
                   "s_mov_b64 exec, %6\n\tbuffer_load_dwordx4 %1, %4, %9, 0 offen offset:1024 sc1\n\t"
                   "s_mov_b64 exec, %7\n\tbuffer_load_dwordx4 %2, %4, %9, 0 offen offset:2048 sc1\n\t"
                   "s_mov_b64 exec, %8\n\tbuffer_load_dwordx4 %3, %4, %9, 0 offen offset:3072 sc1\n\t"
                   "s_mov_b64 exec, -1"
                   : "+v"(pv[4 * m]), "+v"(pv[4 * m + 1]), "+v"(pv[4 * m + 2]), "+v"(pv[4 * m + 3])
                   : "v"(po + m * 16384), "s"(pm[4 * m]), "s"(pm[4 * m + 1]), "s"(pm[4 * m + 2]), "s"(pm[4 * m + 3]), "s"(srd) : "memory");
    }
  }
  asm volatile("s_waitcnt vmcnt(0)" : "+v"(pv[0]), "+v"(pv[1]), "+v"(pv[2]), "+v"(pv[3]), "+v"(pv[4]), "+v"(pv[5]), "+v"(pv[6]), "+v"(pv[7]),
               "+v"(pv[8]), "+v"(pv[9]), "+v"(pv[10]), "+v"(pv[11]), "+v"(pv[12]), "+v"(pv[13]), "+v"(pv[14]), "+v"(pv[15]) :: "memory");
  STAMP(9 + w);
  float y[16];
#pragma unroll
  for (int c = 0; c < 16; ++c) y[c] = 0.f;
#pragma unroll
  for (int m = 0; m < 4; ++m) {
    f32x4 o = __builtin_bit_cast(f32x4, pv[4 * m]) * wt[4 * m];
#pragma unroll
    for (int sx = 1; sx < 4; ++sx) o += __builtin_bit_cast(f32x4, pv[4 * m + sx]) * wt[4 * m + sx];
    const unsigned o01 = cvtpk(o[0], o[1]), o23 = cvtpk(o[2], o[3]);
#pragma unroll
    for (int hf = 0; hf < 2; ++hf)
#pragma unroll
      for (int c = 0; c < 4; ++c) {
        y[8 * hf + 2 * c] = dot2(wv[(m * 4 + 0) * 2 + hf][c], o01, y[8 * hf + 2 * c]);
        y[8 * hf + 2 * c + 1] = dot2(wv[(m * 4 + 1) * 2 + hf][c], o01, y[8 * hf + 2 * c + 1]);
        y[8 * hf + 2 * c] = dot2(wv[(m * 4 + 2) * 2 + hf][c], o23, y[8 * hf + 2 * c]);
        y[8 * hf + 2 * c + 1] = dot2(wv[(m * 4 + 3) * 2 + hf][c], o23, y[8 * hf + 2 * c + 1]);
      }
  }
#pragma unroll
  for (int c = 0; c < 8; ++c) y[c] = swap32add(y[c], y[c + 8]);
#pragma unroll
  for (int c = 0; c < 4; ++c) y[c] = swap16add(y[c], y[c + 4]);
#pragma unroll
  for (int c = 0; c < 2; ++c) { const float t1 = y[c] + dpp<XOR8>(y[c]), t2 = y[c + 2] + dpp<XOR8>(y[c + 2]); y[c] = (lane & 8) ? t2 : t1; }
  { const float t1 = y[0] + dpp<XOR7>(y[0]), t2 = y[1] + dpp<XOR7>(y[1]); y[0] = (lane & 4) ? t2 : t1; }
  y[0] += dpp<XOR2>(y[0]); y[0] += dpp<XOR1>(y[0]);
  if ((lane & 3) == 0) ((LAS float*)(L + R_OFF))[w * 16 + (lane >> 2)] = y[0];
  LDSW();
  unsigned old = 0;
  if (lane == 0) old = __hip_atomic_fetch_add((LAS unsigned*)(L + C_OFF) + 11, 1u, __ATOMIC_RELAXED, __HIP_MEMORY_SCOPE_WORKGROUP);
  if (RFL(old) == NCONS - 1) {
    asm volatile("" ::: "memory");
    if (lane < 8) {
      const LAS f32x2* r = (const LAS f32x2*)(L + R_OFF) + lane;
      const f32x2 v = (r[0] + r[8]) + (r[16] + r[24]);
      *(GAS unsigned*)(out + b * D + ct * 16 + 2 * lane) = cvtpk(v[0], v[1]);
    }
    if (bid == 0 && lane == 0) __hip_atomic_store((GAS unsigned*)(ws + WS_E), tag, RLX_AGENT);
    STAMP(13);
  }
}

extern "C" void kernel_launch(void* const* d_in, const int* in_sizes, int n_in, void* d_out, int out_size, void* d_ws, size_t ws_size, hipStream_t stream) {
  (void)hipFuncSetAttribute((const void*)k_fused, hipFuncAttributeMaxDynamicSharedMemorySize, LDS_BYTES);
  k_fused<<<B * HKV * NSPLIT, (1 + NCONS) * 64, LDS_BYTES, stream>>>((const unsigned short*)d_in[0], (const unsigned short*)d_in[1], (const unsigned short*)d_in[2],
      (const unsigned short*)d_in[3], (const unsigned short*)d_in[4], (unsigned short*)d_out, (unsigned char*)d_ws);
}
```

```cpp
#include <hip/hip_runtime.h>
#include <hip/hip_bf16.h>
#include <stdint.h>

typedef unsigned v4u __attribute__((ext_vector_type(4)));
typedef unsigned v2u __attribute__((ext_vector_type(2)));
typedef float f32x4 __attribute__((ext_vector_type(4)));
typedef float f32x2 __attribute__((ext_vector_type(2)));
typedef short s16x4 __attribute__((ext_vector_type(4)));
typedef __bf16 bf16x8 __attribute__((ext_vector_type(8)));
typedef __bf16 bf16x2 __attribute__((ext_vector_type(2)));
typedef unsigned long long u64;
#define LAS __attribute__((address_space(3)))
#define GAS __attribute__((address_space(1)))
#define RLX_AGENT __ATOMIC_RELAXED, __HIP_MEMORY_SCOPE_AGENT

constexpr int B = 8, D = 448, HQ = 16, HKV = 8, SK = 8192, DH = 256;
constexpr int NSPLIT = 4;
constexpr int KPS = SK / NSPLIT;
#define INFL 2
#define WO_NT 0
#define WOMODE 0
#define K_AUX 2
#define V_AUX 2
#define WQ_AUX 2
constexpr int TILE_B = 16384, NSLOT = 8, NKT = KPS / 32, NT = 2 * NKT;
constexpr int NCONS = 4;
constexpr int SPIN_CAP = 1 << 14;
constexpr int GSPIN_CAP = 1 << 13;
constexpr int S_OFF = NSLOT * TILE_B;
constexpr int P_OFF = S_OFF + 2 * KPS * 4;
constexpr int Q_OFF = P_OFF + NKT * 128;
constexpr int R_OFF = Q_OFF + 1024;
constexpr int C_OFF = R_OFF + 256;
constexpr int LDS_BYTES = C_OFF + 256;
constexpr int WS_E = 0;
constexpr int WS_QG = 4096;
constexpr int WS_ST = WS_QG + 8 * 2048 * 8;
constexpr int WS_PO = WS_ST + 8 * 16 * 4 * 16;
constexpr int WS_BYTES = WS_PO + 8 * 16 * 4 * 256 * 4;

__device__ __forceinline__ unsigned cvtpk(float lo, float hi) { f32x2 v = {lo, hi}; return __builtin_bit_cast(unsigned, __builtin_convertvector(v, bf16x2)); }
__device__ __forceinline__ f32x4 mfma(v4u a, v4u b, f32x4 c) {
  return __builtin_amdgcn_mfma_f32_16x16x32_bf16(__builtin_bit_cast(bf16x8, a), __builtin_bit_cast(bf16x8, b), c, 0, 0, 0);
}
__device__ __forceinline__ float dot2(unsigned k, unsigned q, float acc) {
  return __builtin_amdgcn_fdot2_f32_bf16(__builtin_bit_cast(bf16x2, k), __builtin_bit_cast(bf16x2, q), acc, false);
}
__device__ __forceinline__ v2u trread(LAS unsigned char* p) { return __builtin_bit_cast(v2u, __builtin_amdgcn_ds_read_tr16_b64_v4i16((LAS s16x4*)p)); }
__device__ __forceinline__ float x16max(float x) {
  auto s = __builtin_amdgcn_permlane16_swap(__float_as_uint(x), __float_as_uint(x), false, false);
  x = fmaxf(__uint_as_float(s[0]), __uint_as_float(s[1]));
  auto t = __builtin_amdgcn_permlane32_swap(__float_as_uint(x), __float_as_uint(x), false, false);
  return fmaxf(__uint_as_float(t[0]), __uint_as_float(t[1]));
}
template <int CTRL> __device__ __forceinline__ float dpp(float x) {
  return __builtin_bit_cast(float, __builtin_amdgcn_mov_dpp(__builtin_bit_cast(int, x), CTRL, 0xf, 0xf, true));
}
constexpr int XOR1 = 0xB1, XOR2 = 0x4E, XOR7 = 0x141, XOR8 = 0x128;
__device__ __forceinline__ float swap32add(float a, float b) {
  auto t = __builtin_amdgcn_permlane32_swap(__float_as_uint(a), __float_as_uint(b), false, false);
  return __uint_as_float(t[0]) + __uint_as_float(t[1]);
}
__device__ __forceinline__ float swap16add(float a, float b) {
  auto t = __builtin_amdgcn_permlane16_swap(__float_as_uint(a), __float_as_uint(b), false, false);
  return __uint_as_float(t[0]) + __uint_as_float(t[1]);
}
__device__ __forceinline__ float wavesum(float x) {
  x += dpp<XOR1>(x); x += dpp<XOR2>(x); x += dpp<XOR7>(x); x += dpp<XOR8>(x);
  x = swap16add(x, x); return swap32add(x, x);
}
#define LDSW() asm volatile("s_waitcnt lgkmcnt(0)" ::: "memory")
#define DRAIN() asm volatile("s_waitcnt vmcnt(0)" ::: "memory")
#define RFL(x) __builtin_amdgcn_readfirstlane(x)

__global__ void __launch_bounds__((1 + NCONS) * 64, 1) k_fused(const unsigned short* __restrict__ x, const unsigned short* __restrict__ kc, const unsigned short* __restrict__ vc,
                                              const unsigned short* __restrict__ wq, const unsigned short* __restrict__ wo, unsigned short* __restrict__ out, unsigned char* ws) {
  extern __shared__ __attribute__((aligned(16))) unsigned char lds[];
  LAS unsigned char* L = (LAS unsigned char*)lds;
  volatile LAS unsigned* ctl = (volatile LAS unsigned*)(L + C_OFF);
  const int bid = blockIdx.x;
  const int s = bid & 3, kvh = (bid >> 2) & 7, b = bid >> 5;
  const int lane = threadIdx.x & 63, wave = RFL(threadIdx.x >> 6);
#define STAMP(i)
  if (wave == 0) {
    const unsigned char* kb = (const unsigned char*)kc + (size_t)bid * (KPS * DH * 2);
    const unsigned char* vb = (const unsigned char*)vc + (size_t)bid * (KPS * DH * 2);
    const unsigned hi = lane >> 5, c = lane & 31;
    const unsigned koff = hi * 512 + ((c ^ hi) << 4);
    const unsigned voff = hi * 512 + ((c ^ (hi << 1)) << 4);
    auto issue = [&](int t) {
      const int slot = t & (NSLOT - 1);
      if (t < NKT) {
        const unsigned char* src = kb + (size_t)t * TILE_B;
#pragma unroll
        for (int m = 0; m < 16; ++m)
          __builtin_amdgcn_global_load_lds((const GAS unsigned*)(src + m * 1024 + (koff ^ (((2 * m) & 15) << 4))), (LAS unsigned*)(L + slot * TILE_B + m * 1024), 16, 0, K_AUX);
      } else {
        const unsigned char* src = vb + (size_t)(t - NKT) * TILE_B;
#pragma unroll
        for (int m = 0; m < 16; ++m)
          __builtin_amdgcn_global_load_lds((const GAS unsigned*)(src + m * 1024 + (voff ^ (((4 * m) & 15) << 4))), (LAS unsigned*)(L + slot * TILE_B + m * 1024), 16, 0, V_AUX);
      }
    };
    ((LAS unsigned*)(L + C_OFF))[lane] = 0u;
#pragma unroll
    for (int t = 0; t < INFL; ++t) issue(t);
    LDSW();
    __builtin_amdgcn_s_barrier();
    for (int t = INFL; t < NT; ++t) {
      if (t >= NSLOT) {
        const unsigned needt = (unsigned)(t - NSLOT);
        _Pragma("nounroll") for (int sp = 0; sp < SPIN_CAP * 16; ++sp) {
          const v4u nd = *(volatile LAS v4u*)(L + C_OFF + 16);
          const unsigned mn = min(min(nd.x, nd.y), min(nd.z, nd.w));
          if (RFL(mn) > needt) break;
          __builtin_amdgcn_s_sleep(1);
        }
        asm volatile("" ::: "memory");
      }
      issue(t);
      asm volatile("s_waitcnt vmcnt(%0)" :: "i"(16 * INFL) : "memory");
      ctl[0] = (unsigned)(t - INFL + 1);
    }
#pragma unroll
    for (int r = INFL - 1; r >= 0; --r) {
      asm volatile("s_waitcnt vmcnt(%0)" :: "i"(16 * r) : "memory");
      ctl[0] = (unsigned)(NT - r);
    }
    return;
  }
  const int w = wave - 1;
  const int g = lane >> 4, r16 = lane & 15, head = lane & 1;
  const int qq = (lane >> 2) & 3, pp = lane & 3;
  const __amdgpu_buffer_rsrc_t rs = __builtin_amdgcn_make_buffer_rsrc((void*)ws, 0, WS_BYTES, 0x00020000);
  unsigned eraw = __hip_atomic_load((GAS unsigned*)(ws + WS_E), RLX_AGENT);
  unsigned tag;
  if (w == 0) {
    const int ctile = (bid & 7) * 32 + (bid >> 3);
    const unsigned short* wsrc = wq + (size_t)(lane >> 1) * 4096 + ctile * 16 + (lane & 1) * 8;
#pragma unroll
    for (int j = 0; j < 14; ++j) __builtin_amdgcn_global_load_lds((const GAS unsigned*)(wsrc + (size_t)j * 32 * 4096), (LAS unsigned*)(L + S_OFF + j * 1024), 16, 0, WQ_AUX);
    v4u xf[14];
#pragma unroll
    for (int j = 0; j < 14; ++j) xf[j] = *(const GAS v4u*)(x + (lane & 7) * D + 32 * j + 8 * g);
    __builtin_amdgcn_sched_barrier(0);
    __builtin_amdgcn_s_barrier();
    DRAIN();
    asm volatile("" : "+v"(eraw));
    tag = RFL(eraw) + 1u;
    f32x4 d = {0.f, 0.f, 0.f, 0.f}, d2 = {0.f, 0.f, 0.f, 0.f};
    LAS unsigned char* wb = L + S_OFF + (8 * g + qq) * 32 + pp * 8;
    v4u wf[14];
#pragma unroll
    for (int j = 0; j < 14; ++j) { const v2u lo = trread(wb + j * 1024), hi = trread(wb + j * 1024 + 128); wf[j] = v4u{lo.x, lo.y, hi.x, hi.y}; }
    __builtin_amdgcn_sched_barrier(0);
#pragma unroll
    for (int j = 0; j < 14; j += 2) { d = mfma(wf[j], xf[j], d); d2 = mfma(wf[j + 1], xf[j + 1], d2); }
    d += d2;
    if (r16 < 8) {
      const v4u gr = {cvtpk(d[0], d[1]), tag, cvtpk(d[2], d[3]), tag};
      __builtin_amdgcn_raw_buffer_store_b128(gr, rs, WS_QG + (r16 * 2048 + ctile * 8 + 2 * g) * 8, 0, 16);
    }
    STAMP(3);
    const int qo = WS_QG + (b * 2048 + kvh * 256) * 8 + lane * 16;
    v4u ga, gb;
    _Pragma("nounroll") for (int sp = 0; sp < GSPIN_CAP; ++sp) {
      ga = __builtin_amdgcn_raw_buffer_load_b128(rs, qo, 0, 16);
      gb = __builtin_amdgcn_raw_buffer_load_b128(rs, qo + 1024, 0, 16);
      const bool ok = ga.y == tag && ga.w == tag && gb.y == tag && gb.w == tag;
      if (__all(ok)) break;
      __builtin_amdgcn_s_sleep(2);
    }
    *(LAS v2u*)(L + Q_OFF + lane * 8) = v2u{ga.x, ga.z};
    *(LAS v2u*)(L + Q_OFF + 512 + lane * 8) = v2u{gb.x, gb.z};
    LDSW();
    ctl[10] = 1u;
    STAMP(4);
  } else {
    if (w == 1) {
      const v4u sent = {~0u, ~0u, ~0u, ~0u};
      __builtin_amdgcn_raw_buffer_store_b128(sent, rs, WS_PO + ((b * 16 + kvh * 2) * 4 + s) * 1024 + lane * 16, 0, 16);
      __builtin_amdgcn_raw_buffer_store_b128(sent, rs, WS_PO + ((b * 16 + kvh * 2 + 1) * 4 + s) * 1024 + lane * 16, 0, 16);
    }
    __builtin_amdgcn_s_barrier();
    _Pragma("nounroll") for (int sp = 0; sp < SPIN_CAP * 16 && RFL(ctl[10]) == 0u; ++sp) { __builtin_amdgcn_s_sleep(2); }
    asm volatile("" : "+v"(eraw) :: "memory");
    tag = RFL(eraw) + 1u;
  }
  v4u qf[8];
#pragma unroll
  for (int i = 0; i < 8; ++i) qf[i] = *(const LAS v4u*)(L + Q_OFF + head * 512 + 64 * i + 16 * g);
  auto wait_full = [&](unsigned n) {
    _Pragma("nounroll") for (int sp = 0; sp < SPIN_CAP && RFL(ctl[0]) < n; ++sp) { __builtin_amdgcn_s_sleep(1); }
    asm volatile("" ::: "memory");
  };
  float mx = -INFINITY;
  {
    unsigned ka[4];
#pragma unroll
    for (int i = 0; i < 4; ++i) ka[i] = r16 * 512 + (((4 * i + g) ^ r16) << 4);
    for (int t = w; t < NKT; t += NCONS) {
      wait_full(t + 1);
      LAS unsigned char* tb = L + (t & (NSLOT - 1)) * TILE_B;
      v4u a0[8], a1[8];
#pragma unroll
      for (int i = 0; i < 8; ++i) { a0[i] = *(const LAS v4u*)(tb + ka[i & 3] + (i >> 2) * 256); a1[i] = *(const LAS v4u*)(tb + ka[i & 3] + (i >> 2) * 256 + 8192); }
      LDSW();
      ctl[4 + w] = (unsigned)(t + NCONS < NKT ? t + NCONS : NKT);
      asm volatile("" : "+v"(a0[0]), "+v"(a0[1]), "+v"(a0[2]), "+v"(a0[3]), "+v"(a0[4]), "+v"(a0[5]), "+v"(a0[6]), "+v"(a0[7]),
                        "+v"(a1[0]), "+v"(a1[1]), "+v"(a1[2]), "+v"(a1[3]), "+v"(a1[4]), "+v"(a1[5]), "+v"(a1[6]), "+v"(a1[7]) :: "memory");
      f32x4 d0 = {0.f, 0.f, 0.f, 0.f}, d1 = {0.f, 0.f, 0.f, 0.f};
#pragma unroll
      for (int i = 0; i < 8; ++i) { d0 = mfma(a0[i], qf[i], d0); d1 = mfma(a1[i], qf[i], d1); }
      mx = fmaxf(mx, fmaxf(fmaxf(fmaxf(d0[0], d0[1]), fmaxf(d0[2], d0[3])), fmaxf(fmaxf(d1[0], d1[1]), fmaxf(d1[2], d1[3]))));
      if (r16 < 4) {
        const f32x4 d = (r16 & 2) ? d1 : d0;
        *(LAS f32x4*)(L + S_OFF + head * (KPS * 4) + (t * 32 + (r16 & 2) * 8 + 4 * g) * 4) = d;
      }
    }
  }
  mx = x16max(mx);
  if (lane < 2) ((LAS float*)(L + C_OFF))[16 + w * 2 + lane] = mx;
  LDSW();
  if (lane == 0) __hip_atomic_fetch_add((LAS unsigned*)(L + C_OFF) + 8, 1u, __ATOMIC_RELAXED, __HIP_MEMORY_SCOPE_WORKGROUP);
  _Pragma("nounroll") for (int sp = 0; sp < SPIN_CAP && RFL(ctl[8]) < NCONS; ++sp) { __builtin_amdgcn_s_sleep(1); }
  asm volatile("" ::: "memory");
  constexpr float C2 = 0.0625f * 1.4426950408889634f;
  {
    const f32x4 ma = *(const LAS f32x4*)(L + C_OFF + 64), mb = *(const LAS f32x4*)(L + C_OFF + 80);
    const float M0 = fmaxf(fmaxf(ma[0], ma[2]), fmaxf(mb[0], mb[2])) * C2, M1 = fmaxf(fmaxf(ma[1], ma[3]), fmaxf(mb[1], mb[3])) * C2;
    const int tt = w * 64 + lane, tile = tt >> 2, gg = tt & 3;
    float sum[2];
#pragma unroll
    for (int h = 0; h < 2; ++h) {
      const f32x4 sa = *(const LAS f32x4*)(L + S_OFF + h * (KPS * 4) + (tile * 32 + 4 * gg) * 4);
      const f32x4 sb = *(const LAS f32x4*)(L + S_OFF + h * (KPS * 4) + (tile * 32 + 16 + 4 * gg) * 4);
      const float mc = h ? M1 : M0;
      float p[8];
#pragma unroll
      for (int j = 0; j < 4; ++j) { p[j] = __builtin_amdgcn_exp2f(fmaf(sa[j], C2, -mc)); p[4 + j] = __builtin_amdgcn_exp2f(fmaf(sb[j], C2, -mc)); }
      sum[h] = ((p[0] + p[1]) + (p[2] + p[3])) + ((p[4] + p[5]) + (p[6] + p[7]));
      v4u pk = {cvtpk(p[0], p[1]), cvtpk(p[2], p[3]), cvtpk(p[4], p[5]), cvtpk(p[6], p[7])};
      *(LAS v4u*)(L + P_OFF + tile * 128 + gg * 32 + h * 16) = pk;
    }
    sum[0] = wavesum(sum[0]); sum[1] = wavesum(sum[1]);
    if (lane < 2) ((LAS float*)(L + C_OFF))[24 + w * 2 + lane] = lane ? sum[1] : sum[0];
    LDSW();
    if (lane == 0) __hip_atomic_fetch_add((LAS unsigned*)(L + C_OFF) + 9, 1u, __ATOMIC_RELAXED, __HIP_MEMORY_SCOPE_WORKGROUP);
    _Pragma("nounroll") for (int sp = 0; sp < SPIN_CAP && RFL(ctl[9]) < NCONS; ++sp) { __builtin_amdgcn_s_sleep(1); }
    asm volatile("" ::: "memory");
    if (w == 1) {
      DRAIN();
      if (lane < 2) {
        const f32x4 la = *(const LAS f32x4*)(L + C_OFF + 96), lb = *(const LAS f32x4*)(L + C_OFF + 112);
        const float Ls = lane ? ((la[1] + la[3]) + (lb[1] + lb[3])) : ((la[0] + la[2]) + (lb[0] + lb[2]));
        const v4u gr = {__float_as_uint(lane ? M1 : M0), tag, __float_as_uint(Ls), tag};
        __builtin_amdgcn_raw_buffer_store_b128(gr, rs, WS_ST + ((b * 16 + kvh * 2 + lane) * 4 + s) * 16, 0, 16);
      }
    }
  }
  const bool has_out = (bid & 7) < 7;
  const int ct = has_out ? (bid & 7) * 4 + ((bid >> 3) & 3) : 0;
  f32x4 acc[4];
#pragma unroll
  for (int u = 0; u < 4; ++u) acc[u] = f32x4{0.f, 0.f, 0.f, 0.f};
  v4u wv[32];
  float wt[16];
  {
    const int key = 4 * g + qq;
    unsigned va[4];
#pragma unroll
    for (int u = 0; u < 4; ++u) va[u] = key * 512 + (((8 * w + 2 * u + (pp >> 1)) ^ (2 * (key & 7))) << 4) + (pp & 1) * 8;
    const unsigned pa = P_OFF + g * 32 + head * 16;
    auto vtile = [&](int t) {
      wait_full(NKT + t + 1);
      LAS unsigned char* tb = L + ((NKT + t) & (NSLOT - 1)) * TILE_B;
      v4u pf = *(const LAS v4u*)(L + pa + t * 128);
      v4u av[4];
#pragma unroll
      for (int u = 0; u < 4; ++u) { const v2u lo = trread(tb + va[u]), hi = trread(tb + va[u] + 8192); av[u] = v4u{lo.x, lo.y, hi.x, hi.y}; }
      LDSW();
      ctl[4 + w] = (unsigned)(NKT + t + 1);
      asm volatile("" : "+v"(av[0]), "+v"(av[1]), "+v"(av[2]), "+v"(av[3]), "+v"(pf) :: "memory");
#pragma unroll
      for (int u = 0; u < 4; ++u) acc[u] = mfma(av[u], pf, acc[u]);
    };
    const unsigned short* wop = wo + (size_t)(w * 256 + 4 * lane) * D + ct * 16;
#pragma unroll
    for (int t = 0; t < 32; ++t) {
      const GAS v4u* p = (const GAS v4u*)(wop + (size_t)((t >> 3) * 1024 + ((t >> 1) & 3)) * D + (t & 1) * 8);
      wv[t] = WO_NT ? __builtin_nontemporal_load(p) : *p;
      __builtin_amdgcn_sched_barrier(0);
      vtile(t);
      __builtin_amdgcn_sched_barrier(0);
    }
#pragma nounroll
    for (int t = 32; t < 48; ++t) vtile(t);
    {
      v4u st;
      const int so = WS_ST + ((b * 16 + w + 4 * ((lane >> 2) & 3)) * 4 + (lane & 3)) * 16;
      _Pragma("nounroll") for (int sp = 0; sp < GSPIN_CAP; ++sp) {
        st = __builtin_amdgcn_raw_buffer_load_b128(rs, so, 0, 16);
        if (__all(st.y == tag && st.w == tag)) break;
        __builtin_amdgcn_s_sleep(8);
      }
      const float ms = __uint_as_float(st.x), ls = __uint_as_float(st.z);
      float M = fmaxf(ms, dpp<XOR1>(ms)); M = fmaxf(M, dpp<XOR2>(M));
      const float e = __builtin_amdgcn_exp2f(ms - M);
      float Lt = e * ls; Lt += dpp<XOR1>(Lt); Lt += dpp<XOR2>(Lt);
      const float wgt = e / Lt;
#pragma unroll
      for (int i = 0; i < 16; ++i) wt[i] = __builtin_bit_cast(float, __builtin_amdgcn_readlane(__builtin_bit_cast(int, wgt), i));
#pragma unroll
      for (int m = 0; m < 4; ++m)
#pragma unroll
        for (int j = 0; j < 2; ++j)
#pragma unroll
          for (int hf = 0; hf < 2; ++hf) {
            const v4u ra = wv[(m * 4 + 2 * j) * 2 + hf], rb = wv[(m * 4 + 2 * j + 1) * 2 + hf];
            v4u ev, od;
#pragma unroll
            for (int c = 0; c < 4; ++c) { ev[c] = __builtin_amdgcn_perm(rb[c], ra[c], 0x05040100u); od[c] = __builtin_amdgcn_perm(rb[c], ra[c], 0x07060302u); }
            asm volatile("" : "+v"(ev), "+v"(od));
            wv[(m * 4 + 2 * j) * 2 + hf] = ev; wv[(m * 4 + 2 * j + 1) * 2 + hf] = od;
          }
      __builtin_amdgcn_sched_barrier(0);
    }
#pragma nounroll
    for (int t = 48; t < NKT; ++t) vtile(t);
  }
  STAMP(5 + w);
  {
    const int u = (r16 >> 1) & 3;
    const f32x4 a01 = (u & 1) ? acc[1] : acc[0], a23 = (u & 1) ? acc[3] : acc[2];
    const f32x4 a = (u & 2) ? a23 : a01;
    v4u o = __builtin_bit_cast(v4u, a);
#pragma unroll
    for (int c = 0; c < 4; ++c) o[c] = min(o[c], 0xFFFFFFFEu);
    if (r16 < 8) __builtin_amdgcn_raw_buffer_store_b128(o, rs, WS_PO + ((b * 16 + kvh * 2 + head) * 4 + s) * 1024 + (64 * w + 16 * u + 4 * g) * 4, 0, 16);
  }
  if (!has_out) return;
  v4u pv[16];
  u64 pm[16];
  const int po = WS_PO + (b * 16 + w) * 4096 + lane * 16;
  typedef int v4i __attribute__((ext_vector_type(4)));
  const u64 wsa = (u64)ws;
  const v4i srd = {(int)(unsigned)wsa, (int)((unsigned)(wsa >> 32) & 0xffffu), WS_BYTES, 0x00020000};
#pragma unroll
  for (int m = 0; m < 4; ++m) {
    asm volatile("buffer_load_dwordx4 %0, %4, %5, 0 offen sc1\n\t"
                 "buffer_load_dwordx4 %1, %4, %5, 0 offen offset:1024 sc1\n\t"
                 "buffer_load_dwordx4 %2, %4, %5, 0 offen offset:2048 sc1\n\t"
                 "buffer_load_dwordx4 %3, %4, %5, 0 offen offset:3072 sc1"
                 : "=&v"(pv[4 * m]), "=&v"(pv[4 * m + 1]), "=&v"(pv[4 * m + 2]), "=&v"(pv[4 * m + 3])
                 : "v"(po + m * 16384), "s"(srd) : "memory");
  }
  int lastn = 16;
  _Pragma("nounroll") for (int sp = 0; sp < GSPIN_CAP; ++sp) {
    asm volatile("s_waitcnt vmcnt(0)" : "+v"(pv[0]), "+v"(pv[1]), "+v"(pv[2]), "+v"(pv[3]), "+v"(pv[4]), "+v"(pv[5]), "+v"(pv[6]), "+v"(pv[7]),
                 "+v"(pv[8]), "+v"(pv[9]), "+v"(pv[10]), "+v"(pv[11]), "+v"(pv[12]), "+v"(pv[13]), "+v"(pv[14]), "+v"(pv[15]) :: "memory");
    u64 any = 0;
#pragma unroll
    for (int i = 0; i < 16; ++i) {
      const unsigned mxv = max(max(pv[i].x, pv[i].y), max(pv[i].z, pv[i].w));
      pm[i] = __builtin_amdgcn_ballot_w64(mxv == ~0u);
      any |= pm[i];
    }
    if (!any) break;
    int npend = 0;
#pragma unroll
    for (int i = 0; i < 16; ++i) npend += pm[i] ? 1 : 0;
    const bool thin = npend > 4 && npend == lastn;
    lastn = npend;
    if (thin) {
      bool seen = false;
#pragma unroll
      for (int i = 0; i < 16; ++i) { const u64 t = pm[i]; pm[i] = seen ? 0ull : t; seen = seen || t; }
      __builtin_amdgcn_s_sleep(8);
    }
#pragma unroll
    for (int m = 0; m < 4; ++m) {
      asm volatile("s_mov_b64 exec, %5\n\tbuffer_load_dwordx4 %0, %4, %9, 0 offen sc1\n\t"
                   "s_mov_b64 exec, %6\n\tbuffer_load_dwordx4 %1, %4, %9, 0 offen offset:1024 sc1\n\t"
                   "s_mov_b64 exec, %7\n\tbuffer_load_dwordx4 %2, %4, %9, 0 offen offset:2048 sc1\n\t"
                   "s_mov_b64 exec, %8\n\tbuffer_load_dwordx4 %3, %4, %9, 0 offen offset:3072 sc1\n\t"
                   "s_mov_b64 exec, -1"
                   : "+v"(pv[4 * m]), "+v"(pv[4 * m + 1]), "+v"(pv[4 * m + 2]), "+v"(pv[4 * m + 3])
                   : "v"(po + m * 16384), "s"(pm[4 * m]), "s"(pm[4 * m + 1]), "s"(pm[4 * m + 2]), "s"(pm[4 * m + 3]), "s"(srd) : "memory");
    }
  }
  asm volatile("s_waitcnt vmcnt(0)" : "+v"(pv[0]), "+v"(pv[1]), "+v"(pv[2]), "+v"(pv[3]), "+v"(pv[4]), "+v"(pv[5]), "+v"(pv[6]), "+v"(pv[7]),
               "+v"(pv[8]), "+v"(pv[9]), "+v"(pv[10]), "+v"(pv[11]), "+v"(pv[12]), "+v"(pv[13]), "+v"(pv[14]), "+v"(pv[15]) :: "memory");
  STAMP(9 + w);
  float y[16];
#pragma unroll
  for (int c = 0; c < 16; ++c) y[c] = 0.f;
#pragma unroll
  for (int m = 0; m < 4; ++m) {
    f32x4 o = __builtin_bit_cast(f32x4, pv[4 * m]) * wt[4 * m];
#pragma unroll
    for (int sx = 1; sx < 4; ++sx) o += __builtin_bit_cast(f32x4, pv[4 * m + sx]) * wt[4 * m + sx];
    const unsigned o01 = cvtpk(o[0], o[1]), o23 = cvtpk(o[2], o[3]);
#pragma unroll
    for (int hf = 0; hf < 2; ++hf)
#pragma unroll
      for (int c = 0; c < 4; ++c) {
        y[8 * hf + 2 * c] = dot2(wv[(m * 4 + 0) * 2 + hf][c], o01, y[8 * hf + 2 * c]);
        y[8 * hf + 2 * c + 1] = dot2(wv[(m * 4 + 1) * 2 + hf][c], o01, y[8 * hf + 2 * c + 1]);
        y[8 * hf + 2 * c] = dot2(wv[(m * 4 + 2) * 2 + hf][c], o23, y[8 * hf + 2 * c]);
        y[8 * hf + 2 * c + 1] = dot2(wv[(m * 4 + 3) * 2 + hf][c], o23, y[8 * hf + 2 * c + 1]);
      }
  }
#pragma unroll
  for (int c = 0; c < 8; ++c) y[c] = swap32add(y[c], y[c + 8]);
#pragma unroll
  for (int c = 0; c < 4; ++c) y[c] = swap16add(y[c], y[c + 4]);
#pragma unroll
  for (int c = 0; c < 2; ++c) { const float t1 = y[c] + dpp<XOR8>(y[c]), t2 = y[c + 2] + dpp<XOR8>(y[c + 2]); y[c] = (lane & 8) ? t2 : t1; }
  { const float t1 = y[0] + dpp<XOR7>(y[0]), t2 = y[1] + dpp<XOR7>(y[1]); y[0] = (lane & 4) ? t2 : t1; }
  y[0] += dpp<XOR2>(y[0]); y[0] += dpp<XOR1>(y[0]);
  if ((lane & 3) == 0) ((LAS float*)(L + R_OFF))[w * 16 + (lane >> 2)] = y[0];
  LDSW();
  unsigned old = 0;
  if (lane == 0) old = __hip_atomic_fetch_add((LAS unsigned*)(L + C_OFF) + 11, 1u, __ATOMIC_RELAXED, __HIP_MEMORY_SCOPE_WORKGROUP);
  if (RFL(old) == NCONS - 1) {
    asm volatile("" ::: "memory");
    if (lane < 8) {
      const LAS f32x2* r = (const LAS f32x2*)(L + R_OFF) + lane;
      const f32x2 v = (r[0] + r[8]) + (r[16] + r[24]);
      *(GAS unsigned*)(out + b * D + ct * 16 + 2 * lane) = cvtpk(v[0], v[1]);
    }
    if (bid == 0 && lane == 0) __hip_atomic_store((GAS unsigned*)(ws + WS_E), tag, RLX_AGENT);
    STAMP(13);
  }
}

extern "C" void kernel_launch(void* const* d_in, const int* in_sizes, int n_in, void* d_out, int out_size, void* d_ws, size_t ws_size, hipStream_t stream) {
  (void)hipFuncSetAttribute((const void*)k_fused, hipFuncAttributeMaxDynamicSharedMemorySize, LDS_BYTES);
  k_fused<<<B * HKV * NSPLIT, (1 + NCONS) * 64, LDS_BYTES, stream>>>((const unsigned short*)d_in[0], (const unsigned short*)d_in[1], (const unsigned short*)d_in[2],
      (const unsigned short*)d_in[3], (const unsigned short*)d_in[4], (unsigned short*)d_out, (unsigned char*)d_ws);
}
```

```cpp
#include <hip/hip_runtime.h>
#include <hip/hip_bf16.h>
#include <stdint.h>

typedef unsigned v4u __attribute__((ext_vector_type(4)));
typedef unsigned v2u __attribute__((ext_vector_type(2)));
typedef float f32x4 __attribute__((ext_vector_type(4)));
typedef float f32x2 __attribute__((ext_vector_type(2)));
typedef short s16x4 __attribute__((ext_vector_type(4)));
typedef __bf16 bf16x8 __attribute__((ext_vector_type(8)));
typedef __bf16 bf16x2 __attribute__((ext_vector_type(2)));
typedef unsigned long long u64;
#define LAS __attribute__((address_space(3)))
#define GAS __attribute__((address_space(1)))
#define RLX_AGENT __ATOMIC_RELAXED, __HIP_MEMORY_SCOPE_AGENT

constexpr int B = 8, D = 448, HQ = 16, HKV = 8, SK = 8192, DH = 256;
constexpr int NSPLIT = 4;
constexpr int KPS = SK / NSPLIT;
#define INFL 3
#define TAU 888
#define NPRE 3
#define WQ_AUX 2
#define WO_AUX 2
#define WO_AT 128
#define ST_AT 40
#define D1 12
#define D2 24
#define WARM 0
#define TH1 50
#define TH2 200
#define W0 16
#define NOPRIO 0
#define NOSWZ 0
#define SLEEPC 1
#define PAD ""
#define LEAD 30
#define PK0 4
#define PKG 16
#define PKN 8
#define PHS 16
#define PKD 3
#define TOUCH 1
constexpr int TILE_B = 16384, NSLOT = 8, NKT = KPS / 32, NT = 2 * NKT + 1;
constexpr int NCONS = 4;
constexpr int SPIN_CAP = 1 << 14;
constexpr int GSPIN_CAP = 1 << 13;
constexpr int S_OFF = NSLOT * TILE_B;
constexpr int P_OFF = S_OFF + 2 * KPS * 4;
constexpr int Q_OFF = P_OFF + NKT * 128;
constexpr int C_OFF = Q_OFF + 1024;
constexpr int LDS_BYTES = C_OFF + 256;
constexpr int WS_E = 0;
constexpr int WS_PH = 2 << 20;
constexpr int WS_QG = 4096;
constexpr int WS_ST = WS_QG + 8 * 2048 * 8;
constexpr int WS_PO = WS_ST + 8 * 16 * 4 * 16;
constexpr int WS_Y2 = WS_PO + 8 * 16 * 4 * 1024;
constexpr int WS_BYTES = WS_Y2 + 8 * 7 * 4 * 4096;

__device__ __forceinline__ unsigned cvtpk(float lo, float hi) { f32x2 v = {lo, hi}; return __builtin_bit_cast(unsigned, __builtin_convertvector(v, bf16x2)); }
__device__ __forceinline__ f32x4 mfma(v4u a, v4u b, f32x4 c) {
  return __builtin_amdgcn_mfma_f32_16x16x32_bf16(__builtin_bit_cast(bf16x8, a), __builtin_bit_cast(bf16x8, b), c, 0, 0, 0);
}
__device__ __forceinline__ v2u trread(LAS unsigned char* p) { return __builtin_bit_cast(v2u, __builtin_amdgcn_ds_read_tr16_b64_v4i16((LAS s16x4*)p)); }
__device__ __forceinline__ float x16max(float x) {
  auto s = __builtin_amdgcn_permlane16_swap(__float_as_uint(x), __float_as_uint(x), false, false);
  x = fmaxf(__uint_as_float(s[0]), __uint_as_float(s[1]));
  auto t = __builtin_amdgcn_permlane32_swap(__float_as_uint(x), __float_as_uint(x), false, false);
  return fmaxf(__uint_as_float(t[0]), __uint_as_float(t[1]));
}
template <int CTRL> __device__ __forceinline__ float dpp(float x) {
  return __builtin_bit_cast(float, __builtin_amdgcn_mov_dpp(__builtin_bit_cast(int, x), CTRL, 0xf, 0xf, true));
}
constexpr int XOR1 = 0xB1, XOR2 = 0x4E, XOR7 = 0x141, XOR8 = 0x128;
__device__ __forceinline__ float swap32add(float a, float b) {
  auto t = __builtin_amdgcn_permlane32_swap(__float_as_uint(a), __float_as_uint(b), false, false);
  return __uint_as_float(t[0]) + __uint_as_float(t[1]);
}
__device__ __forceinline__ float swap16add(float a, float b) {
  auto t = __builtin_amdgcn_permlane16_swap(__float_as_uint(a), __float_as_uint(b), false, false);
  return __uint_as_float(t[0]) + __uint_as_float(t[1]);
}
__device__ __forceinline__ float wavesum(float x) {
  x += dpp<XOR1>(x); x += dpp<XOR2>(x); x += dpp<XOR7>(x); x += dpp<XOR8>(x);
  x = swap16add(x, x); return swap32add(x, x);
}
#define LDSW() asm volatile("s_waitcnt lgkmcnt(0)" ::: "memory")
#define DRAIN() asm volatile("s_waitcnt vmcnt(0)" ::: "memory")
#define PIN16(a) asm volatile("" : "+v"(a[0]), "+v"(a[1]), "+v"(a[2]), "+v"(a[3]), "+v"(a[4]), "+v"(a[5]), "+v"(a[6]), "+v"(a[7]), \
                        "+v"(a[8]), "+v"(a[9]), "+v"(a[10]), "+v"(a[11]), "+v"(a[12]), "+v"(a[13]), "+v"(a[14]), "+v"(a[15]) :: "memory")
#define RFL(x) __builtin_amdgcn_readfirstlane(x)
#define LD_SC1(off) __builtin_amdgcn_raw_buffer_load_b128(rs, (off), 0, 16)
#define ST_SC1(v, off) __builtin_amdgcn_raw_buffer_store_b128((v), rs, (off), 0, 16)

__global__ void __launch_bounds__((2 + NCONS) * 64, 1) k_fused(const unsigned short* __restrict__ x, const unsigned short* __restrict__ kc, const unsigned short* __restrict__ vc,
                                              const unsigned short* __restrict__ wq, const unsigned short* __restrict__ wo, unsigned short* __restrict__ out, unsigned char* ws) {
  extern __shared__ __attribute__((aligned(16))) unsigned char lds[];
  LAS unsigned char* L = (LAS unsigned char*)lds;
  volatile LAS unsigned* ctl = (volatile LAS unsigned*)(L + C_OFF);
  const int bid = blockIdx.x;
  const int s = bid & 3, kvh = (bid >> 2) & 7, b = bid >> 5;
  const int lane = threadIdx.x & 63, wave = RFL(threadIdx.x >> 6);
  const bool has_out = (bid & 7) < 7;
  const int lc = has_out ? (bid & 7) : 0, rg = bid >> 3;
#define STAMP(i)
#define STAMP_FLUSH()
  if (wave == 0) {
    const unsigned tl0 = (unsigned)__builtin_amdgcn_s_memrealtime();
    __builtin_amdgcn_s_setprio(3);
    const u64 kb = (u64)kc + (u64)bid * (KPS * DH * 2), vb = (u64)vc + (u64)bid * (KPS * DH * 2);
    const unsigned hi = lane >> 5, c = lane & 31;
    unsigned vo[16];
    const unsigned koff = hi * 512 + ((c ^ hi) << 4);
    const unsigned voff = hi * 512 + ((c ^ (hi << 1)) << 4);
#pragma unroll
    for (int m = 0; m < 16; ++m) vo[m] = (NOSWZ ? lane * 16 : (koff ^ (((2 * m) & 15) << 4))) + (m >> 2) * 4096;
    const unsigned ldsbase = (unsigned)(size_t)L;
    auto issue = [&](u64 src, int t) {
      const unsigned dst = ldsbase + (t & (NSLOT - 1)) * TILE_B;
      unsigned keep;
      asm volatile("s_mov_b32 %0, m0\n\t"
                   "s_mov_b32 m0, %18\n\ts_nop 0\n\t"
                   "global_load_lds_dwordx4 %1, %17 nt\n\t" PAD "global_load_lds_dwordx4 %2, %17 offset:1024 nt\n\t" PAD ""
                   "global_load_lds_dwordx4 %3, %17 offset:2048 nt\n\t" PAD "global_load_lds_dwordx4 %4, %17 offset:3072 nt\n\t" PAD ""
                   "s_add_u32 m0, %18, 0x1000\n\ts_nop 0\n\t"
                   "global_load_lds_dwordx4 %5, %17 nt\n\t" PAD "global_load_lds_dwordx4 %6, %17 offset:1024 nt\n\t" PAD ""
                   "global_load_lds_dwordx4 %7, %17 offset:2048 nt\n\t" PAD "global_load_lds_dwordx4 %8, %17 offset:3072 nt\n\t" PAD ""
                   "s_add_u32 m0, %18, 0x2000\n\ts_nop 0\n\t"
                   "global_load_lds_dwordx4 %9, %17 nt\n\t" PAD "global_load_lds_dwordx4 %10, %17 offset:1024 nt\n\t" PAD ""
                   "global_load_lds_dwordx4 %11, %17 offset:2048 nt\n\t" PAD "global_load_lds_dwordx4 %12, %17 offset:3072 nt\n\t" PAD ""
                   "s_add_u32 m0, %18, 0x3000\n\ts_nop 0\n\t"
                   "global_load_lds_dwordx4 %13, %17 nt\n\t" PAD "global_load_lds_dwordx4 %14, %17 offset:1024 nt\n\t" PAD ""
                   "global_load_lds_dwordx4 %15, %17 offset:2048 nt\n\t" PAD "global_load_lds_dwordx4 %16, %17 offset:3072 nt\n\t" PAD ""
                   "s_mov_b32 m0, %0"
                   : "=&s"(keep)
                   : "v"(vo[0]), "v"(vo[1]), "v"(vo[2]), "v"(vo[3]), "v"(vo[4]), "v"(vo[5]), "v"(vo[6]), "v"(vo[7]),
                     "v"(vo[8]), "v"(vo[9]), "v"(vo[10]), "v"(vo[11]), "v"(vo[12]), "v"(vo[13]), "v"(vo[14]), "v"(vo[15]), "s"(src), "s"(dst)
                   : "memory", "scc");
    };
    issue(kb, 0);
    issue(kb + TILE_B, 1);
    ((LAS unsigned*)(L + C_OFF))[lane] = 0u;
    LDSW();
    __builtin_amdgcn_s_barrier();
    int pub = 0;
    unsigned phi = tl0;
    auto step = [&](u64 src, int t) {
      if (t >= NSLOT) {
        const unsigned needt = (unsigned)(t - NSLOT);
        _Pragma("nounroll") for (int sp = 0; sp < SPIN_CAP * 16; ++sp) {
          const v4u nd = *(volatile LAS v4u*)(L + C_OFF + 16);
          const unsigned mn = min(min(nd.x, nd.y), min(nd.z, nd.w));
          if (RFL(mn) > needt) break;
          __builtin_amdgcn_s_sleep(1);
        }
        asm volatile("" ::: "memory");
      }
      const v2u rf = *(volatile LAS v2u*)(L + C_OFF + 48);
      const unsigned rvalid = RFL(rf.x), ref = RFL(rf.y);
      const int e = rvalid ? (int)(phi - ref) : 0;
      const unsigned tt = ((unsigned)t * TAU) >> 4;
      int landed, adj;
      if (e > TH2) { asm volatile("s_waitcnt vmcnt(47)" ::: "memory"); landed = t - 3; adj = 31; }
      else if (e > TH1) { asm volatile("s_waitcnt vmcnt(32)" ::: "memory"); landed = t - 2; adj = 16; }
      else {
        asm volatile("s_waitcnt vmcnt(%0)" :: "i"(W0) : "memory"); landed = W0 <= 16 ? t - 1 : t - 2; adj = W0 - 16;
        if (e < 0 && t < 2 * NKT) { const unsigned due = ref + tt - LEAD; _Pragma("nounroll") for (int sp = 0; sp < 64 && (int)((unsigned)__builtin_amdgcn_s_memrealtime() - due) < 0; ++sp) __builtin_amdgcn_s_sleep(1); }
      }
      issue(src, t);
      phi = (unsigned)__builtin_amdgcn_s_memrealtime() - tt + (((unsigned)adj * TAU) >> 8);
      ctl[1] = phi;
      if (landed > pub) { pub = landed; ctl[0] = (unsigned)pub; }
    };
#pragma nounroll
    for (int t = 2; t < NKT; ++t) step(kb + (u64)t * TILE_B, t);
#pragma unroll
    for (int m = 0; m < 16; ++m) vo[m] = (NOSWZ ? lane * 16 : (voff ^ (((4 * m) & 15) << 4))) + (m >> 2) * 4096;
#pragma nounroll
    for (int t = NKT; t < 2 * NKT; ++t) step(vb + (u64)(t - NKT) * TILE_B, t);
#pragma unroll
    for (int m = 0; m < 16; ++m) vo[m] = (m * 8 + (lane >> 3)) * (D * 2) + (lane & 7) * 16 + 4096 - (m & 3) * 1024;
    step((u64)wo + (u64)(rg * 128) * (D * 2) + lc * 128 - 4096, 2 * NKT);
#pragma unroll
    for (int r = 3; r >= 0; --r) {
      asm volatile("s_waitcnt vmcnt(%0)" :: "i"(16 * r) : "memory");
      if (NT - r > pub) { pub = NT - r; ctl[0] = (unsigned)pub; }
    }
    return;
  }
  if (wave == 1 + NCONS) {
    __builtin_amdgcn_s_barrier();
    _Pragma("nounroll") for (int sp = 0; sp < SPIN_CAP && RFL(ctl[2]) == 0u; ++sp) { __builtin_amdgcn_s_sleep(4); }
    const unsigned tag = RFL(ctl[3]);
#pragma nounroll
    for (int k = PK0; k < 2 * NKT - 2 - PKD; k += PKG) {
      _Pragma("nounroll") for (int sp = 0; sp < SPIN_CAP && RFL(ctl[0]) < (unsigned)k; ++sp) { __builtin_amdgcn_s_sleep(4); }
      const unsigned mine = RFL(ctl[1]);
      if (lane == 0) __hip_atomic_store((GAS u64*)(ws + WS_PH) + bid * PHS, ((u64)tag << 32) | mine, RLX_AGENT);
      _Pragma("nounroll") for (int sp = 0; sp < SPIN_CAP && RFL(ctl[0]) < (unsigned)(k + PKD); ++sp) { __builtin_amdgcn_s_sleep(4); }
      u64 o = 0;
      if (lane < PKN) o = __hip_atomic_load((GAS u64*)(ws + WS_PH) + ((bid & ~(PKN - 1)) + lane) * PHS, RLX_AGENT);
      const bool ok = (unsigned)(o >> 32) == tag && lane < PKN;
      const float dsum = wavesum(ok ? (float)(int)((unsigned)o - mine) : 0.f);
      const int n = __builtin_popcountll(__builtin_amdgcn_ballot_w64(ok));
      if (n >= PKN / 2) { const unsigned ref = mine + (unsigned)(int)(dsum / (float)n); *(volatile LAS v2u*)(L + C_OFF + 48) = v2u{1u, ref}; }
    }
    return;
  }
  const int w = wave - 1;
  const int g = lane >> 4, r16 = lane & 15, head = lane & 1;
  const int qq = (lane >> 2) & 3, pp = lane & 3;
  const __amdgpu_buffer_rsrc_t rs = __builtin_amdgcn_make_buffer_rsrc((void*)ws, 0, WS_BYTES, 0x00020000);
  unsigned eraw = __hip_atomic_load((GAS unsigned*)(ws + WS_E), RLX_AGENT);
  unsigned tag;
  auto wait_full = [&](unsigned n) {
    _Pragma("nounroll") for (int sp = 0; sp < SPIN_CAP && RFL(ctl[0]) < n; ++sp) { __builtin_amdgcn_s_sleep(SLEEPC); }
    asm volatile("" ::: "memory");
  };
  unsigned ka[4];
#pragma unroll
  for (int i = 0; i < 4; ++i) ka[i] = r16 * 512 + (((4 * i + g) ^ r16) << 4);
  v4u pre[NPRE][16];
  if (w == 0) {
    const int ctile = ((bid & 7) + 8 * (bid >> 5)) * 4 + ((bid >> 3) & 3);
    const unsigned short* wsrc = wq + (size_t)(lane >> 1) * 4096 + ctile * 16 + (lane & 1) * 8;
#pragma unroll
    for (int j = 0; j < 14; ++j) __builtin_amdgcn_global_load_lds((const GAS unsigned*)(wsrc + (size_t)j * 32 * 4096), (LAS unsigned*)(L + S_OFF + j * 1024), 16, 0, WQ_AUX);
    v4u xf[14];
#pragma unroll
    for (int j = 0; j < 14; ++j) xf[j] = *(const GAS v4u*)(x + (lane & 7) * D + 32 * j + 8 * g);
    __builtin_amdgcn_sched_barrier(0);
    __builtin_amdgcn_s_barrier();
    ctl[4] = (unsigned)(3 * NPRE);
    DRAIN();
    asm volatile("" : "+v"(eraw));
    tag = RFL(eraw) + 1u;
    *(volatile LAS v2u*)(L + C_OFF + 8) = v2u{1u, tag};
    f32x4 d = {0.f, 0.f, 0.f, 0.f}, d2 = {0.f, 0.f, 0.f, 0.f};
    LAS unsigned char* wb = L + S_OFF + (8 * g + qq) * 32 + pp * 8;
    v4u wf[14];
#pragma unroll
    for (int j = 0; j < 14; ++j) { const v2u lo = trread(wb + j * 1024), hi = trread(wb + j * 1024 + 128); wf[j] = v4u{lo.x, lo.y, hi.x, hi.y}; }
    __builtin_amdgcn_sched_barrier(0);
#pragma unroll
    for (int j = 0; j < 14; j += 2) { d = mfma(wf[j], xf[j], d); d2 = mfma(wf[j + 1], xf[j + 1], d2); }
    d += d2;
    if (r16 < 8) {
      const v4u gr = {cvtpk(d[0], d[1]), tag, cvtpk(d[2], d[3]), tag};
      ST_SC1(gr, WS_QG + (r16 * 2048 + ctile * 8 + 2 * g) * 8);
    }
    STAMP(3);
    const int qo = WS_QG + (b * 2048 + kvh * 256) * 8 + lane * 16;
    v4u ga, gb;
    _Pragma("nounroll") for (int sp = 0; sp < GSPIN_CAP; ++sp) {
      ga = LD_SC1(qo);
      gb = LD_SC1(qo + 1024);
      const bool ok = ga.y == tag && ga.w == tag && gb.y == tag && gb.w == tag;
      if (__all(ok)) break;
      __builtin_amdgcn_s_sleep(2);
      asm volatile("" ::: "memory");
    }
    *(LAS v2u*)(L + Q_OFF + lane * 8) = v2u{ga.x, ga.z};
    *(LAS v2u*)(L + Q_OFF + 512 + lane * 8) = v2u{gb.x, gb.z};
    LDSW();
    ctl[10] = 1u;
    STAMP(4);
  } else {
    __builtin_amdgcn_s_barrier();
#pragma unroll
    for (int j = 0; j < NPRE; ++j) {
      const int t = (w - 1) + 3 * j;
      wait_full(t + 1);
      LAS unsigned char* tb = L + (t & (NSLOT - 1)) * TILE_B;
#pragma unroll
      for (int i = 0; i < 8; ++i) { pre[j][i] = *(const LAS v4u*)(tb + ka[i & 3] + (i >> 2) * 256); pre[j][8 + i] = *(const LAS v4u*)(tb + ka[i & 3] + (i >> 2) * 256 + 8192); }
      LDSW();
      ctl[4 + w] = (unsigned)(j + 1 < NPRE ? t + 3 : 3 * NPRE + w);
      PIN16(pre[j]);
    }
    _Pragma("nounroll") for (int sp = 0; sp < SPIN_CAP * 16 && RFL(ctl[10]) == 0u; ++sp) { __builtin_amdgcn_s_sleep(2); }
    asm volatile("" : "+v"(eraw) :: "memory");
    tag = RFL(eraw) + 1u;
  }
  v4u qf[8];
#pragma unroll
  for (int i = 0; i < 8; ++i) qf[i] = *(const LAS v4u*)(L + Q_OFF + head * 512 + 64 * i + 16 * g);
  float mx = -INFINITY;
  auto qk = [&](const v4u (&a)[16], int t) {
    f32x4 d0 = {0.f, 0.f, 0.f, 0.f}, d1 = {0.f, 0.f, 0.f, 0.f};
#pragma unroll
    for (int i = 0; i < 8; ++i) { d0 = mfma(a[i], qf[i], d0); d1 = mfma(a[8 + i], qf[i], d1); }
    mx = fmaxf(mx, fmaxf(fmaxf(fmaxf(d0[0], d0[1]), fmaxf(d0[2], d0[3])), fmaxf(fmaxf(d1[0], d1[1]), fmaxf(d1[2], d1[3]))));
    if (r16 < 4) {
      const f32x4 d = (r16 & 2) ? d1 : d0;
      *(LAS f32x4*)(L + S_OFF + head * (KPS * 4) + (t * 32 + (r16 & 2) * 8 + 4 * g) * 4) = d;
    }
  };
  if (w > 0) {
#pragma unroll
    for (int j = 0; j < NPRE; ++j) qk(pre[j], (w - 1) + 3 * j);
  }
  unsigned touch = 0;
#pragma nounroll
  for (int t = 3 * NPRE + w; t < NKT; t += NCONS) {
    if (t == 33) {
      const unsigned char* tp = lane == 0 ? (const unsigned char*)vc + (size_t)bid * (KPS * DH * 2) : lane == 1 ? (const unsigned char*)out : lane == 2 ? (const unsigned char*)wo + (size_t)rg * 128 * (D * 2) : ws + WS_Y2;
      touch = __builtin_nontemporal_load((const GAS unsigned*)tp);
    }
    wait_full(t + 1);
    LAS unsigned char* tb = L + (t & (NSLOT - 1)) * TILE_B;
    v4u a[16];
#pragma unroll
    for (int i = 0; i < 8; ++i) { a[i] = *(const LAS v4u*)(tb + ka[i & 3] + (i >> 2) * 256); a[8 + i] = *(const LAS v4u*)(tb + ka[i & 3] + (i >> 2) * 256 + 8192); }
    LDSW();
    ctl[4 + w] = (unsigned)(t + NCONS < NKT ? t + NCONS : NKT);
    PIN16(a);
    qk(a, t);
  }
  mx = x16max(mx);
  if (lane < 2) ((LAS float*)(L + C_OFF))[16 + w * 2 + lane] = mx;
  asm volatile("" :: "v"(touch));
  LDSW();
  if (lane == 0) __hip_atomic_fetch_add((LAS unsigned*)(L + C_OFF) + 8, 1u, __ATOMIC_RELAXED, __HIP_MEMORY_SCOPE_WORKGROUP);
  _Pragma("nounroll") for (int sp = 0; sp < SPIN_CAP && RFL(ctl[8]) < NCONS; ++sp) { __builtin_amdgcn_s_sleep(1); }
  asm volatile("" ::: "memory");
  constexpr float C2 = 0.0625f * 1.4426950408889634f;
  {
    const f32x4 ma = *(const LAS f32x4*)(L + C_OFF + 64), mb = *(const LAS f32x4*)(L + C_OFF + 80);
    const float M0 = fmaxf(fmaxf(ma[0], ma[2]), fmaxf(mb[0], mb[2])) * C2, M1 = fmaxf(fmaxf(ma[1], ma[3]), fmaxf(mb[1], mb[3])) * C2;
    const int tt = w * 64 + lane, tile = tt >> 2, gg = tt & 3;
    float sum[2];
#pragma unroll
    for (int h = 0; h < 2; ++h) {
      const f32x4 sa = *(const LAS f32x4*)(L + S_OFF + h * (KPS * 4) + (tile * 32 + 4 * gg) * 4);
      const f32x4 sb = *(const LAS f32x4*)(L + S_OFF + h * (KPS * 4) + (tile * 32 + 16 + 4 * gg) * 4);
      const float mc = h ? M1 : M0;
      float p[8];
#pragma unroll
      for (int j = 0; j < 4; ++j) { p[j] = __builtin_amdgcn_exp2f(fmaf(sa[j], C2, -mc)); p[4 + j] = __builtin_amdgcn_exp2f(fmaf(sb[j], C2, -mc)); }
      sum[h] = ((p[0] + p[1]) + (p[2] + p[3])) + ((p[4] + p[5]) + (p[6] + p[7]));
      v4u pk = {cvtpk(p[0], p[1]), cvtpk(p[2], p[3]), cvtpk(p[4], p[5]), cvtpk(p[6], p[7])};
      *(LAS v4u*)(L + P_OFF + tile * 128 + gg * 32 + h * 16) = pk;
    }
    sum[0] = wavesum(sum[0]); sum[1] = wavesum(sum[1]);
    if (lane < 2) ((LAS float*)(L + C_OFF))[24 + w * 2 + lane] = lane ? sum[1] : sum[0];
    LDSW();
    if (lane == 0) __hip_atomic_fetch_add((LAS unsigned*)(L + C_OFF) + 9, 1u, __ATOMIC_RELAXED, __HIP_MEMORY_SCOPE_WORKGROUP);
    _Pragma("nounroll") for (int sp = 0; sp < SPIN_CAP && RFL(ctl[9]) < NCONS; ++sp) { __builtin_amdgcn_s_sleep(1); }
    asm volatile("" ::: "memory");
    if (w == 1 && lane < 2) {
      const f32x4 la = *(const LAS f32x4*)(L + C_OFF + 96), lb = *(const LAS f32x4*)(L + C_OFF + 112);
      const float Ls = lane ? ((la[1] + la[3]) + (lb[1] + lb[3])) : ((la[0] + la[2]) + (lb[0] + lb[2]));
      const v4u gr = {__float_as_uint(lane ? M1 : M0), tag, __float_as_uint(Ls), tag};
      ST_SC1(gr, WS_ST + ((b * 16 + kvh * 2 + lane) * 4 + s) * 16);
    }
  }
  f32x4 acc[4];
#pragma unroll
  for (int u = 0; u < 4; ++u) acc[u] = f32x4{0.f, 0.f, 0.f, 0.f};
  float oscale;
  v4u wa[4];
  const int key = 4 * g + qq;
  auto wotile = [&]() {
    wait_full(WO_AT + 1);
    LAS unsigned char* tb = L + (WO_AT & (NSLOT - 1)) * TILE_B + (32 * w + 8 * g + qq) * 128 + pp * 8;
#pragma unroll
    for (int n = 0; n < 4; ++n) { const v2u lo = trread(tb + n * 32), hi = trread(tb + n * 32 + 512); wa[n] = v4u{lo.x, lo.y, hi.x, hi.y}; }
    LDSW();
    ctl[4 + w] = (unsigned)(WO_AT + 1);
    asm volatile("" : "+v"(wa[0]), "+v"(wa[1]), "+v"(wa[2]), "+v"(wa[3]) :: "memory");
  };
  {
    unsigned va[4];
#pragma unroll
    for (int u = 0; u < 4; ++u) va[u] = key * 512 + (((8 * w + 2 * u + (pp >> 1)) ^ (2 * (key & 7))) << 4) + (pp & 1) * 8;
    const unsigned pa = P_OFF + g * 32 + head * 16;
    auto vtile = [&](int t, int pos) {
      wait_full(pos + 1);
      LAS unsigned char* tb = L + (pos & (NSLOT - 1)) * TILE_B;
      v4u pf = *(const LAS v4u*)(L + pa + t * 128);
      v4u av[4];
#pragma unroll
      for (int u = 0; u < 4; ++u) { const v2u lo = trread(tb + va[u]), hi = trread(tb + va[u] + 8192); av[u] = v4u{lo.x, lo.y, hi.x, hi.y}; }
      LDSW();
      ctl[4 + w] = (unsigned)(pos + 1);
      asm volatile("" : "+v"(av[0]), "+v"(av[1]), "+v"(av[2]), "+v"(av[3]), "+v"(pf) :: "memory");
#pragma unroll
      for (int u = 0; u < 4; ++u) acc[u] = mfma(av[u], pf, acc[u]);
    };
#pragma nounroll
    for (int t = 0; t < ST_AT; ++t) vtile(t, NKT + t);
    {
      v4u sc;
      _Pragma("nounroll") for (int sp = 0; sp < GSPIN_CAP; ++sp) {
        sc = LD_SC1(WS_ST + (b * 16 + kvh * 2) * 64 + (lane & 7) * 16);
        if (__all(sc.y == tag && sc.w == tag)) break;
        __builtin_amdgcn_s_sleep(8);
        asm volatile("" ::: "memory");
      }
      const float ms = __uint_as_float(sc.x), ls = __uint_as_float(sc.z);
      float M = fmaxf(ms, dpp<XOR1>(ms)); M = fmaxf(M, dpp<XOR2>(M));
      const float e = __builtin_amdgcn_exp2f(ms - M);
      float Lt = e * ls; Lt += dpp<XOR1>(Lt); Lt += dpp<XOR2>(Lt);
      const int wgt = __builtin_bit_cast(int, e / Lt);
      const int f0 = __builtin_amdgcn_readlane(wgt, s), f1 = __builtin_amdgcn_readlane(wgt, 4 + s);
      oscale = __builtin_bit_cast(float, head ? f1 : f0);
      asm volatile("" : "+v"(oscale));
    }
#pragma nounroll
    for (int t = ST_AT; t < WO_AT - NKT; ++t) vtile(t, NKT + t);
    if (WO_AT < 2 * NKT) {
      wotile();
#pragma nounroll
      for (int t = WO_AT - NKT; t < NKT; ++t) vtile(t, NKT + t + 1);
    }
  }
  STAMP(5 + w);
  {
    const int u = (r16 >> 1) & 3;
    const f32x4 a01 = (u & 1) ? acc[1] : acc[0], a23 = (u & 1) ? acc[3] : acc[2];
    const f32x4 a = ((u & 2) ? a23 : a01) * oscale;
    const v4u o = {cvtpk(a[0], a[1]), tag, cvtpk(a[2], a[3]), tag};
    if (r16 < 8) ST_SC1(o, WS_PO + ((b * 16 + kvh * 2 + head) * 4 + s) * 1024 + (64 * w + 16 * u + 4 * g) * 4);
  }
  if (!has_out) { STAMP_FLUSH(); return; }
  typedef int v4i __attribute__((ext_vector_type(4)));
  const u64 wsa = (u64)ws;
  const v4i srd = {(int)(unsigned)wsa, (int)((unsigned)(wsa >> 32) & 0xffffu), WS_BYTES, 0x00020000};
  v4u p0, p1, p2, p3;
  auto gather_issue = [&](int voff) {
    asm volatile("buffer_load_dwordx4 %0, %4, %5, 0 offen sc1\n\t"
                 "buffer_load_dwordx4 %1, %4, %5, 0 offen offset:1024 sc1\n\t"
                 "buffer_load_dwordx4 %2, %4, %5, 0 offen offset:2048 sc1\n\t"
                 "buffer_load_dwordx4 %3, %4, %5, 0 offen offset:3072 sc1"
                 : "=&v"(p0), "=&v"(p1), "=&v"(p2), "=&v"(p3) : "v"(voff), "s"(srd) : "memory");
  };
  auto gather_wait = [&](int voff) {
    _Pragma("nounroll") for (int sp = 0; sp < GSPIN_CAP; ++sp) {
      asm volatile("s_waitcnt vmcnt(0)" : "+v"(p0), "+v"(p1), "+v"(p2), "+v"(p3) :: "memory");
      const u64 m0 = __builtin_amdgcn_ballot_w64(p0.y != tag || p0.w != tag), m1 = __builtin_amdgcn_ballot_w64(p1.y != tag || p1.w != tag);
      const u64 m2 = __builtin_amdgcn_ballot_w64(p2.y != tag || p2.w != tag), m3 = __builtin_amdgcn_ballot_w64(p3.y != tag || p3.w != tag);
      if (!(m0 | m1 | m2 | m3)) break;
      if (sp > 16) __builtin_amdgcn_s_sleep(8);
      asm volatile("s_mov_b64 exec, %5\n\tbuffer_load_dwordx4 %0, %4, %9, 0 offen sc1\n\t"
                   "s_mov_b64 exec, %6\n\tbuffer_load_dwordx4 %1, %4, %9, 0 offen offset:1024 sc1\n\t"
                   "s_mov_b64 exec, %7\n\tbuffer_load_dwordx4 %2, %4, %9, 0 offen offset:2048 sc1\n\t"
                   "s_mov_b64 exec, %8\n\tbuffer_load_dwordx4 %3, %4, %9, 0 offen offset:3072 sc1\n\t"
                   "s_mov_b64 exec, -1"
                   : "+v"(p0), "+v"(p1), "+v"(p2), "+v"(p3) : "v"(voff), "s"(m0), "s"(m1), "s"(m2), "s"(m3), "s"(srd) : "memory");
    }
    asm volatile("s_waitcnt vmcnt(0)" : "+v"(p0), "+v"(p1), "+v"(p2), "+v"(p3) :: "memory");
  };
  {
    const int po = WS_PO + ((lane >> 3) * 16 + (rg >> 1)) * 4096 + ((rg & 1) * 128 + 32 * w) * 4 + (lane & 7) * 16;
    __builtin_amdgcn_s_sleep(D1);
    gather_issue(po);
    if (WO_AT == 2 * NKT) wotile();
    gather_wait(po);
    STAMP(9 + w);
    LAS unsigned char* tb = L + S_OFF + w * 4096;
    *(LAS v2u*)(tb + lane * 8) = v2u{p0.x, p0.z}; *(LAS v2u*)(tb + 512 + lane * 8) = v2u{p1.x, p1.z};
    *(LAS v2u*)(tb + 1024 + lane * 8) = v2u{p2.x, p2.z}; *(LAS v2u*)(tb + 1536 + lane * 8) = v2u{p3.x, p3.z};
    LDSW();
    const v4u ba = *(const LAS v4u*)(tb + r16 * 64 + g * 16), bb = *(const LAS v4u*)(tb + 1024 + r16 * 64 + g * 16);
    f32x4 y[4];
#pragma unroll
    for (int n = 0; n < 4; ++n) y[n] = mfma(wa[n], ba, f32x4{0.f, 0.f, 0.f, 0.f});
#pragma unroll
    for (int n = 0; n < 4; ++n) y[n] = mfma(wa[n], bb, y[n]);
#pragma unroll
    for (int n = 0; n < 4; ++n) *(LAS f32x4*)(L + S_OFF + w * 4096 + (r16 >> 3) * 2048 + n * 512 + (r16 & 7) * 64 + g * 16) = y[n];
    LDSW();
    __builtin_amdgcn_s_barrier();
    asm volatile("" ::: "memory");
    const LAS f32x2* rp = (const LAS f32x2*)(L + S_OFF + w * 512 + lane * 8);
    const f32x2 v = ((rp[0] + rp[256]) + (rp[512] + rp[768])) + ((rp[1024] + rp[1280]) + (rp[1536] + rp[1792]));
    const v4u o = {__float_as_uint(v[0]), tag, __float_as_uint(v[1]), tag};
    ST_SC1(o, WS_Y2 + ((((lane >> 3) * 7 + lc) * 4 + w) * 32 + rg) * 128 + (lane & 7) * 16);
  }
  if (w != 0) { STAMP_FLUSH(); return; }
  STAMP(14);
  {
    const int nt = (bid >> 3) & 3;
    const int yo = WS_Y2 + ((b * 7 + lc) * 4 + nt) * 4096 + lane * 16;
    __builtin_amdgcn_s_sleep(D2);
    gather_issue(yo);
    gather_wait(yo);
    STAMP(15);
    float v0 = (__uint_as_float(p0.x) + __uint_as_float(p1.x)) + (__uint_as_float(p2.x) + __uint_as_float(p3.x));
    float v1 = (__uint_as_float(p0.z) + __uint_as_float(p1.z)) + (__uint_as_float(p2.z) + __uint_as_float(p3.z));
    v0 += dpp<XOR8>(v0); v1 += dpp<XOR8>(v1);
    v0 = swap16add(v0, v0); v1 = swap16add(v1, v1);
    v0 = swap32add(v0, v0); v1 = swap32add(v1, v1);
    if (lane < 8) *(GAS unsigned*)(out + b * D + lc * 64 + nt * 16 + 2 * lane) = cvtpk(v0, v1);
    if (bid == 0 && lane == 0) __hip_atomic_store((GAS unsigned*)(ws + WS_E), tag, RLX_AGENT);
    STAMP(13);
    STAMP_FLUSH();
  }
}

extern "C" void kernel_launch(void* const* d_in, const int* in_sizes, int n_in, void* d_out, int out_size, void* d_ws, size_t ws_size, hipStream_t stream) {
  (void)hipFuncSetAttribute((const void*)k_fused, hipFuncAttributeMaxDynamicSharedMemorySize, LDS_BYTES);
  k_fused<<<B * HKV * NSPLIT, (2 + NCONS) * 64, LDS_BYTES, stream>>>((const unsigned short*)d_in[0], (const unsigned short*)d_in[1], (const unsigned short*)d_in[2],
      (const unsigned short*)d_in[3], (const unsigned short*)d_in[4], (unsigned short*)d_out, (unsigned char*)d_ws);
}
```

```cpp
#include <hip/hip_runtime.h>
#include <hip/hip_bf16.h>
#include <stdint.h>

typedef unsigned v4u __attribute__((ext_vector_type(4)));
typedef unsigned v2u __attribute__((ext_vector_type(2)));
typedef int v4i __attribute__((ext_vector_type(4)));
typedef float f32x4 __attribute__((ext_vector_type(4)));
typedef float f32x2 __attribute__((ext_vector_type(2)));
typedef short s16x4 __attribute__((ext_vector_type(4)));
typedef __bf16 bf16x8 __attribute__((ext_vector_type(8)));
typedef __bf16 bf16x2 __attribute__((ext_vector_type(2)));
typedef unsigned long long u64;
#define LAS __attribute__((address_space(3)))
#define GAS __attribute__((address_space(1)))
#define RLX_AGENT __ATOMIC_RELAXED, __HIP_MEMORY_SCOPE_AGENT

constexpr int B = 8, D = 448, HKV = 8, SK = 8192, DH = 256;
constexpr int NSPLIT = 4;
constexpr int KPS = SK / NSPLIT;
#define TAU 888
#define NPRE 3
#define ST_AT 40
#define D1 0
#define D2 16
#define TH1 25
#define TH2 120
#define LEAD 30
#define PK0 4
#define PKG 24
#define PKD 3
#define QD 550
#define WO_AT 126
#define DRY_AT 90
#define W0 16
#define ENDW 0
#define ENDV 1000
#define G1 150
#define G2 0
#define MAP2 1
#define TOUCH 1
constexpr int TILE_B = 16384, NSLOT = 8, NKT = KPS / 32, NT = 2 * NKT;
constexpr int NCONS = 4, NWAVE = 7;
constexpr int WAIT_BUDGET = 1 << 22;
constexpr int S_OFF = NSLOT * TILE_B;
constexpr int X_OFF = S_OFF + 2 * KPS * 4;
constexpr int P_OFF = X_OFF + 2048;
constexpr int Q_OFF = P_OFF + NKT * 128;
constexpr int C_OFF = Q_OFF + 1024;
constexpr int LDS_BYTES = C_OFF + 256;
constexpr int WS_E = 0;
constexpr int WS_QG = 4096;
constexpr int WS_ST = WS_QG + 8 * 2048 * 8;
constexpr int WS_PO = WS_ST + 8 * 16 * 4 * 16;
constexpr int WS_Y2 = WS_PO + 8 * 16 * 4 * 1024;
constexpr int WS_PH = WS_Y2 + 8 * 7 * 4 * 4096;
constexpr int WS_BYTES = WS_PH + 256 * 128;

__device__ __forceinline__ unsigned cvtpk(float lo, float hi) { f32x2 v = {lo, hi}; return __builtin_bit_cast(unsigned, __builtin_convertvector(v, bf16x2)); }
__device__ __forceinline__ f32x4 mfma(v4u a, v4u b, f32x4 c) {
  return __builtin_amdgcn_mfma_f32_16x16x32_bf16(__builtin_bit_cast(bf16x8, a), __builtin_bit_cast(bf16x8, b), c, 0, 0, 0);
}
__device__ __forceinline__ v2u trread(LAS unsigned char* p) { return __builtin_bit_cast(v2u, __builtin_amdgcn_ds_read_tr16_b64_v4i16((LAS s16x4*)p)); }
__device__ __forceinline__ float x16max(float x) {
  auto s = __builtin_amdgcn_permlane16_swap(__float_as_uint(x), __float_as_uint(x), false, false);
  x = fmaxf(__uint_as_float(s[0]), __uint_as_float(s[1]));
  auto t = __builtin_amdgcn_permlane32_swap(__float_as_uint(x), __float_as_uint(x), false, false);
  return fmaxf(__uint_as_float(t[0]), __uint_as_float(t[1]));
}
template <int CTRL> __device__ __forceinline__ float dpp(float x) {
  return __builtin_bit_cast(float, __builtin_amdgcn_mov_dpp(__builtin_bit_cast(int, x), CTRL, 0xf, 0xf, true));
}
constexpr int XOR1 = 0xB1, XOR2 = 0x4E, XOR7 = 0x141, XOR8 = 0x128;
__device__ __forceinline__ float swap32add(float a) {
  auto t = __builtin_amdgcn_permlane32_swap(__float_as_uint(a), __float_as_uint(a), false, false);
  return __uint_as_float(t[0]) + __uint_as_float(t[1]);
}
__device__ __forceinline__ float swap16add(float a) {
  auto t = __builtin_amdgcn_permlane16_swap(__float_as_uint(a), __float_as_uint(a), false, false);
  return __uint_as_float(t[0]) + __uint_as_float(t[1]);
}
__device__ __forceinline__ float wavesum(float x) {
  x += dpp<XOR1>(x); x += dpp<XOR2>(x); x += dpp<XOR7>(x); x += dpp<XOR8>(x);
  return swap32add(swap16add(x));
}
__device__ __forceinline__ unsigned now() { return (unsigned)__builtin_amdgcn_s_memrealtime(); }
#define LDSW() asm volatile("s_waitcnt lgkmcnt(0)" ::: "memory")
#define DRAIN() asm volatile("s_waitcnt vmcnt(0)" ::: "memory")
#define PIN16(a) asm volatile("" : "+v"(a[0]), "+v"(a[1]), "+v"(a[2]), "+v"(a[3]), "+v"(a[4]), "+v"(a[5]), "+v"(a[6]), "+v"(a[7]), \
                        "+v"(a[8]), "+v"(a[9]), "+v"(a[10]), "+v"(a[11]), "+v"(a[12]), "+v"(a[13]), "+v"(a[14]), "+v"(a[15]) :: "memory")
#define RFL(x) __builtin_amdgcn_readfirstlane(x)
#define LD_SC1(off) __builtin_amdgcn_raw_buffer_load_b128(rs, (off), 0, 16)
#define ST_SC1(v, off) __builtin_amdgcn_raw_buffer_store_b128((v), rs, (off), 0, 16)
#define SPIN _Pragma("nounroll") for (int sp = 0;
#define WAIT(cond, cost) _Pragma("nounroll") for (; bud > 0 && (cond); bud -= (cost))

__global__ void __launch_bounds__(NWAVE * 64, 1) k_fused(const unsigned short* __restrict__ x, const unsigned short* __restrict__ kc, const unsigned short* __restrict__ vc,
                                              const unsigned short* __restrict__ wq, const unsigned short* __restrict__ wo, unsigned short* __restrict__ out, unsigned char* ws) {
  extern __shared__ __attribute__((aligned(16))) unsigned char lds[];
  LAS unsigned char* L = (LAS unsigned char*)lds;
  volatile LAS unsigned* ctl = (volatile LAS unsigned*)(L + C_OFF);
  const int bid = blockIdx.x;
  const int lane = threadIdx.x & 63, wave = RFL(threadIdx.x >> 6);
  int bud = WAIT_BUDGET;
#define STAMP(i)
#define STAMP_FLUSH()
  unsigned o0 = (lane >> 5) * 512 + (((lane & 31) ^ ((lane >> 5) << 1)) << 4);
  unsigned o1 = o0 ^ 64, o2 = o0 ^ 128, o3 = o0 ^ 192;
  const unsigned ldsbase = (unsigned)(size_t)L;
  u64 src = (u64)kc + (u64)bid * (KPS * DH * 2);
#define DMA4(S) "global_load_lds_dwordx4 %1, " S " nt\n\tglobal_load_lds_dwordx4 %2, " S " offset:1024 nt\n\t" \
                "global_load_lds_dwordx4 %3, " S " offset:2048 nt\n\tglobal_load_lds_dwordx4 %4, " S " offset:3072 nt\n\t"
  {
    unsigned keep, cnt, c0 = o0, c1 = o1, c2 = o2, c3 = o3;
    asm volatile("s_cmp_lg_u32 %8, 0\n\ts_cbranch_scc1 .Lskip%=\n\t"
                 "s_setprio 3\n\t"
                 "s_mov_b32 %0, m0\n\ts_mov_b32 m0, %7\n\ts_mov_b32 %5, 8\n"
                 ".Lloop%=:\n\t" DMA4("%6")
                 "s_add_u32 m0, m0, 0x1000\n\t"
                 "v_add_u32 %1, 0x1000, %1\n\tv_add_u32 %2, 0x1000, %2\n\tv_add_u32 %3, 0x1000, %3\n\tv_add_u32 %4, 0x1000, %4\n\t"
                 "s_sub_u32 %5, %5, 1\n\ts_cmp_lg_u32 %5, 0\n\ts_cbranch_scc1 .Lloop%=\n\t"
                 "s_mov_b32 m0, %0\n"
                 ".Lskip%=:"
                 : "=&s"(keep), "+v"(c0), "+v"(c1), "+v"(c2), "+v"(c3), "=&s"(cnt)
                 : "s"(src), "s"(ldsbase), "s"(wave) : "memory", "scc");
  }
  if (threadIdx.x < 64) ((LAS unsigned*)(L + C_OFF))[lane] = 0u;
  LDSW();
  __builtin_amdgcn_s_barrier();
  if (wave == 0) {
    const u64 vb = (u64)vc + (u64)bid * (KPS * DH * 2);
    src += 2 * TILE_B;
    int pub = 0;
    unsigned phi = 0;
#pragma nounroll
    for (int t = 2; t < NT + 3; ++t) {
      if (t >= NSLOT && t < NT) {
        const unsigned needt = (unsigned)(t - NSLOT);
        WAIT(true, 1) {
          const v4u nd = *(volatile LAS v4u*)(L + C_OFF + 16);
          const unsigned mn = min(min(nd.x, nd.y), min(nd.z, nd.w));
          if (RFL(mn) > needt) break;
          __builtin_amdgcn_s_sleep(1);
        }
        asm volatile("" ::: "memory");
      }
      const v2u rf = *(volatile LAS v2u*)(L + C_OFF + 48);
      const unsigned rvalid = RFL(rf.x), ref = RFL(rf.y);
      const int e = t >= NT ? (t == NT ? TH2 + 1 : t == NT + 1 ? TH1 + 1 : 0) : t >= NT - ENDW ? ENDV : rvalid ? (int)(phi - ref) : 0;
      const unsigned tt = ((unsigned)t * TAU) >> 4;
      const int issued = t < NT ? t : NT;
      if (t == NKT) src = vb;
      int landed; unsigned adj;
      if (e > TH2) { asm volatile("s_waitcnt vmcnt(47)" ::: "memory"); landed = issued - 3; adj = ((47 - W0) * TAU) >> 8; }
      else if (e > TH1) { asm volatile("s_waitcnt vmcnt(32)" ::: "memory"); landed = issued - 2; adj = ((32 - W0) * TAU) >> 8; }
      else if (W0 > 16 && t > NT + 1) { asm volatile("s_waitcnt vmcnt(16)" ::: "memory"); landed = issued - 1; adj = 0; }
      else {
        asm volatile("s_waitcnt vmcnt(%0)" :: "i"(W0) : "memory"); landed = issued - (W0 > 16 ? 2 : 1); adj = 0;
        if (e < 0) { const unsigned due = ref + tt - LEAD; SPIN sp < 64 && (int)(now() - due) < 0; ++sp) __builtin_amdgcn_s_sleep(1); }
      }
      if (t < NT) {
        const unsigned dst = ldsbase + (t & (NSLOT - 1)) * TILE_B;
        const u64 s1 = src + 4096, s2 = src + 8192, s3 = src + 12288;
        unsigned keep;
        asm volatile("s_mov_b32 %0, m0\n\t"
                     "s_mov_b32 m0, %9\n\ts_nop 0\n\t" DMA4("%5")
                     "s_add_u32 m0, %9, 0x1000\n\ts_nop 0\n\t" DMA4("%6")
                     "s_add_u32 m0, %9, 0x2000\n\ts_nop 0\n\t" DMA4("%7")
                     "s_add_u32 m0, %9, 0x3000\n\ts_nop 0\n\t" DMA4("%8")
                     "s_mov_b32 m0, %0"
                     : "=&s"(keep) : "v"(o0), "v"(o1), "v"(o2), "v"(o3), "s"(src), "s"(s1), "s"(s2), "s"(s3), "s"(dst) : "memory", "scc");
        src += TILE_B;
      }
      if (landed > pub) pub = landed;
      phi = now() - tt + adj;
      *(volatile LAS v2u*)(L + C_OFF) = v2u{(unsigned)pub, phi};
    }
    DRAIN();
    ctl[0] = (unsigned)NT;
    return;
  }
  const int s = bid & 3, kvh = (bid >> 2) & 7, b = bid >> 5;
  const bool has_out = (bid >> 5) < 7;
  const int lc = has_out ? (bid >> 5) : 0, rg = (bid & 7) * 4 + ((bid >> 3) & 3);
  const int b3 = bid & 7;
  auto wait_full = [&](unsigned n) {
    WAIT(RFL(ctl[0]) < n, 1) { __builtin_amdgcn_s_sleep(1); }
    asm volatile("" ::: "memory");
  };
  auto wait_full_slow = [&](unsigned n) {
    WAIT(RFL(ctl[0]) < n, 4) { __builtin_amdgcn_s_sleep(4); }
    asm volatile("" ::: "memory");
  };
  if (wave == 5) {
    WAIT(RFL(ctl[2]) == 0u, 4) { __builtin_amdgcn_s_sleep(4); }
    const unsigned tag = RFL(ctl[3]);
#pragma nounroll
    for (int k = PK0; k + PKD < WO_AT; k += PKG) {
      wait_full_slow(k);
      const unsigned mine = RFL(ctl[1]);
      if (lane == 0) __hip_atomic_store((GAS u64*)(ws + WS_PH) + bid * 16, ((u64)tag << 32) | mine, RLX_AGENT);
      wait_full_slow(k + PKD);
      u64 o = 0;
      if (lane < 8) o = __hip_atomic_load((GAS u64*)(ws + WS_PH) + ((bid & ~7) + lane) * 16, RLX_AGENT);
      const bool ok = (unsigned)(o >> 32) == tag && lane < 8;
      const float dsum = wavesum(ok ? (float)(int)((unsigned)o - mine) : 0.f);
      const int n = __builtin_popcountll(__builtin_amdgcn_ballot_w64(ok));
      if (n >= 4) { const unsigned ref = mine + (unsigned)(int)(dsum / (float)n); *(volatile LAS v2u*)(L + C_OFF + 48) = v2u{1u, ref}; }
    }
    const unsigned char* src = (const unsigned char*)wo + (size_t)(rg * 128 + (lane >> 3)) * (D * 2) + lc * 128 + (lane & 7) * 16;
    wait_full_slow(WO_AT);
    WAIT(RFL(ctl[9]) < NCONS, 4) { __builtin_amdgcn_s_sleep(4); }
    asm volatile("" ::: "memory");
#pragma unroll
    for (int m = 0; m < 16; ++m) __builtin_amdgcn_global_load_lds((const GAS unsigned*)(src + m * 8 * (D * 2)), (LAS unsigned*)(L + S_OFF + m * 1024), 16, 0, 2);
    DRAIN();
    ctl[11] = 1u;
    return;
  }
  const __amdgpu_buffer_rsrc_t rs = __builtin_amdgcn_make_buffer_rsrc((void*)ws, 0, WS_BYTES, 0x00020000);
  if (wave == 6) {
    if (DRY_AT == 0) return;
    wait_full_slow(DRY_AT);
    const int yo = WS_Y2 + ((b3 * 7 + lc) * 4 + ((bid >> 3) & 3)) * 4096 + lane * 16;
    const int po = WS_PO + ((b * 16 + kvh * 2) * 4 + s) * 1024 + lane * 16;
    const v4u a0 = LD_SC1(po), a1 = LD_SC1(po + 4096);
    asm volatile("" :: "v"(a0), "v"(a1));
    if (has_out) {
      wait_full_slow(DRY_AT + 4);
      const v4u b0 = LD_SC1(yo), b1 = LD_SC1(yo + 1024), b2 = LD_SC1(yo + 2048), b3 = LD_SC1(yo + 3072);
      asm volatile("" :: "v"(b0), "v"(b1), "v"(b2), "v"(b3));
    }
    return;
  }
  const bool live = true;
  const int w = wave - 1;
  const int g = lane >> 4, r16 = lane & 15, head = lane & 1;
  const int qq = (lane >> 2) & 3, pp = lane & 3;
  unsigned tag = 0;
  f32x4 acc[4];
#pragma unroll
  for (int u = 0; u < 4; ++u) acc[u] = f32x4{0.f, 0.f, 0.f, 0.f};
  float oscale = 0.f;
  unsigned tgate = 0;
  {
    unsigned ka[4];
#pragma unroll
    for (int i = 0; i < 4; ++i) ka[i] = r16 * 512 + (((4 * i + g) ^ (2 * (r16 & 7))) << 4);
    v4u pre[NPRE][16];
    if (w == 0) {
      const unsigned tq0 = now();
      ctl[4] = (unsigned)(3 * NPRE);
      unsigned eraw = __hip_atomic_load((GAS unsigned*)(ws + WS_E), RLX_AGENT);
      const int ctile = ((bid & 7) + 8 * (bid >> 5)) * 4 + ((bid >> 3) & 3);
      const unsigned short* wsrc = wq + (size_t)(lane >> 1) * 4096 + ctile * 16 + (lane & 1) * 8;
#pragma unroll
      for (int j = 0; j < 14; ++j) __builtin_amdgcn_global_load_lds((const GAS unsigned*)(wsrc + (size_t)j * 32 * 4096), (LAS unsigned*)(L + S_OFF + j * 1024), 16, 0, 2);
      v4u xf[14];
#pragma unroll
      for (int j = 0; j < 14; ++j) xf[j] = *(const GAS v4u*)(x + (lane & 7) * D + 32 * j + 8 * g);
      __builtin_amdgcn_sched_barrier(0);
      DRAIN();
      asm volatile("" : "+v"(eraw));
      tag = RFL(eraw) + 1u;
      *(volatile LAS v2u*)(L + C_OFF + 8) = v2u{1u, tag};
      f32x4 d = {0.f, 0.f, 0.f, 0.f}, d2 = {0.f, 0.f, 0.f, 0.f};
      LAS unsigned char* wb = L + S_OFF + (8 * g + qq) * 32 + pp * 8;
      v4u wf[14];
#pragma unroll
      for (int j = 0; j < 14; ++j) { const v2u lo = trread(wb + j * 1024), hi = trread(wb + j * 1024 + 128); wf[j] = v4u{lo.x, lo.y, hi.x, hi.y}; }
      __builtin_amdgcn_sched_barrier(0);
#pragma unroll
      for (int j = 0; j < 14; j += 2) { d = mfma(wf[j], xf[j], d); d2 = mfma(wf[j + 1], xf[j + 1], d2); }
      d += d2;
      if (r16 < 8) {
        const v4u gr = {cvtpk(d[0], d[1]), tag, cvtpk(d[2], d[3]), tag};
        ST_SC1(gr, WS_QG + (r16 * 2048 + ctile * 8 + 2 * g) * 8);
      }
      STAMP(3);
      const int qo = WS_QG + (b * 2048 + kvh * 256) * 8 + lane * 16;
      v4u ga = {0u, 0u, 0u, 0u}, gb = ga;
      SPIN sp < 1024 && (int)(now() - tq0) < QD; ++sp) __builtin_amdgcn_s_sleep(2);
      WAIT(true, 16) {
        ga = LD_SC1(qo);
        gb = LD_SC1(qo + 1024);
        const bool ok = ga.y == tag && ga.w == tag && gb.y == tag && gb.w == tag;
        if (__all(ok)) break;
        __builtin_amdgcn_s_sleep(8);
        asm volatile("" ::: "memory");
      }
      *(LAS v2u*)(L + Q_OFF + lane * 8) = v2u{ga.x, ga.z};
      *(LAS v2u*)(L + Q_OFF + 512 + lane * 8) = v2u{gb.x, gb.z};
      LDSW();
      ctl[10] = 1u;
      STAMP(4);
    } else {
#pragma unroll
      for (int j = 0; j < NPRE; ++j) {
        const int t = (w - 1) + 3 * j;
        wait_full(t + 1);
        LAS unsigned char* tb = L + (t & (NSLOT - 1)) * TILE_B;
#pragma unroll
        for (int i = 0; i < 8; ++i) { pre[j][i] = *(const LAS v4u*)(tb + ka[i & 3] + (i >> 2) * 256); pre[j][8 + i] = *(const LAS v4u*)(tb + ka[i & 3] + (i >> 2) * 256 + 8192); }
        LDSW();
        ctl[4 + w] = (unsigned)(j + 1 < NPRE ? t + 3 : 3 * NPRE + w);
        PIN16(pre[j]);
      }
      WAIT(RFL(ctl[10]) == 0u, 2) { __builtin_amdgcn_s_sleep(2); }
      asm volatile("" ::: "memory");
      tag = RFL(ctl[3]);
    }
    v4u qf[8];
#pragma unroll
    for (int i = 0; i < 8; ++i) qf[i] = *(const LAS v4u*)(L + Q_OFF + head * 512 + 64 * i + 16 * g);
    float mx = -INFINITY;
    auto qk = [&](const v4u (&a)[16], int t) {
      f32x4 d0 = {0.f, 0.f, 0.f, 0.f}, d1 = {0.f, 0.f, 0.f, 0.f};
#pragma unroll
      for (int i = 0; i < 8; ++i) { d0 = mfma(a[i], qf[i], d0); d1 = mfma(a[8 + i], qf[i], d1); }
      mx = fmaxf(mx, fmaxf(fmaxf(fmaxf(d0[0], d0[1]), fmaxf(d0[2], d0[3])), fmaxf(fmaxf(d1[0], d1[1]), fmaxf(d1[2], d1[3]))));
      if (r16 < 4) {
        const f32x4 d = (r16 & 2) ? d1 : d0;
        *(LAS f32x4*)(L + S_OFF + head * (KPS * 4) + (t * 32 + (r16 & 2) * 8 + 4 * g) * 4) = d;
      }
    };
    if (w > 0) {
#pragma unroll
      for (int j = 0; j < NPRE; ++j) qk(pre[j], (w - 1) + 3 * j);
    }
    unsigned touch = 0;
#pragma nounroll
    for (int t = 3 * NPRE + w; t < NKT; t += NCONS) {
      if (t == 33) {
        const unsigned char* tp = lane == 0 ? (const unsigned char*)vc + (size_t)bid * (KPS * DH * 2) : lane == 1 ? (const unsigned char*)out : (const unsigned char*)wo + (size_t)rg * 128 * (D * 2);
        touch = __builtin_nontemporal_load((const GAS unsigned*)tp);
      }
      wait_full(t + 1);
      LAS unsigned char* tb = L + (t & (NSLOT - 1)) * TILE_B;
      v4u a[16];
#pragma unroll
      for (int i = 0; i < 8; ++i) { a[i] = *(const LAS v4u*)(tb + ka[i & 3] + (i >> 2) * 256); a[8 + i] = *(const LAS v4u*)(tb + ka[i & 3] + (i >> 2) * 256 + 8192); }
      LDSW();
      ctl[4 + w] = (unsigned)(t + NCONS < NKT ? t + NCONS : NKT);
      PIN16(a);
      qk(a, t);
    }
    mx = x16max(mx);
    if (lane < 2) ((LAS float*)(L + C_OFF))[16 + w * 2 + lane] = mx;
    asm volatile("" :: "v"(touch));
    LDSW();
    if (lane == 0) __hip_atomic_fetch_add((LAS unsigned*)(L + C_OFF) + 8, 1u, __ATOMIC_RELAXED, __HIP_MEMORY_SCOPE_WORKGROUP);
    WAIT(RFL(ctl[8]) < NCONS, 1) { __builtin_amdgcn_s_sleep(1); }
    asm volatile("" ::: "memory");
    constexpr float C2 = 0.0625f * 1.4426950408889634f;
    {
      const f32x4 ma = *(const LAS f32x4*)(L + C_OFF + 64), mb = *(const LAS f32x4*)(L + C_OFF + 80);
      const float M0 = fmaxf(fmaxf(ma[0], ma[2]), fmaxf(mb[0], mb[2])) * C2, M1 = fmaxf(fmaxf(ma[1], ma[3]), fmaxf(mb[1], mb[3])) * C2;
      const int tt = w * 64 + lane, tile = tt >> 2, gg = tt & 3;
      float sum[2];
#pragma unroll
      for (int h = 0; h < 2; ++h) {
        const f32x4 sa = *(const LAS f32x4*)(L + S_OFF + h * (KPS * 4) + (tile * 32 + 4 * gg) * 4);
        const f32x4 sb = *(const LAS f32x4*)(L + S_OFF + h * (KPS * 4) + (tile * 32 + 16 + 4 * gg) * 4);
        const float mc = h ? M1 : M0;
        float p[8];
#pragma unroll
        for (int j = 0; j < 4; ++j) { p[j] = __builtin_amdgcn_exp2f(fmaf(sa[j], C2, -mc)); p[4 + j] = __builtin_amdgcn_exp2f(fmaf(sb[j], C2, -mc)); }
        sum[h] = ((p[0] + p[1]) + (p[2] + p[3])) + ((p[4] + p[5]) + (p[6] + p[7]));
        v4u pk = {cvtpk(p[0], p[1]), cvtpk(p[2], p[3]), cvtpk(p[4], p[5]), cvtpk(p[6], p[7])};
        *(LAS v4u*)(L + P_OFF + tile * 128 + gg * 32 + h * 16) = pk;
      }
      sum[0] = wavesum(sum[0]); sum[1] = wavesum(sum[1]);
      if (lane < 2) ((LAS float*)(L + C_OFF))[24 + w * 2 + lane] = lane ? sum[1] : sum[0];
      LDSW();
      if (lane == 0) __hip_atomic_fetch_add((LAS unsigned*)(L + C_OFF) + 9, 1u, __ATOMIC_RELAXED, __HIP_MEMORY_SCOPE_WORKGROUP);
      WAIT(RFL(ctl[9]) < NCONS, 1) { __builtin_amdgcn_s_sleep(1); }
      asm volatile("" ::: "memory");
      if (w == 1 && lane < 2) {
        const f32x4 la = *(const LAS f32x4*)(L + C_OFF + 96), lb = *(const LAS f32x4*)(L + C_OFF + 112);
        const float Ls = lane ? ((la[1] + la[3]) + (lb[1] + lb[3])) : ((la[0] + la[2]) + (lb[0] + lb[2]));
        const v4u gr = {__float_as_uint(lane ? M1 : M0), tag, __float_as_uint(Ls), tag};
        ST_SC1(gr, WS_ST + ((b * 16 + kvh * 2 + lane) * 4 + s) * 16);
      }
    }
    const int key = 4 * g + qq;
    unsigned va[4];
#pragma unroll
    for (int u = 0; u < 4; ++u) va[u] = key * 512 + (((8 * w + 2 * u + (pp >> 1)) ^ (2 * (key & 7))) << 4) + (pp & 1) * 8;
    const unsigned pa = P_OFF + g * 32 + head * 16;
#pragma nounroll
    for (int t = 0; t < NKT; ++t) {
      if (t == ST_AT) {
        v4u sc = {0u, 0u, 0u, 0u};
        WAIT(true, 16) {
          sc = LD_SC1(WS_ST + (b * 16 + kvh * 2) * 64 + (lane & 7) * 16);
          if (__all(sc.y == tag && sc.w == tag)) break;
          __builtin_amdgcn_s_sleep(8);
          asm volatile("" ::: "memory");
        }
        const float ms = __uint_as_float(sc.x), ls = __uint_as_float(sc.z);
        float M = fmaxf(ms, dpp<XOR1>(ms)); M = fmaxf(M, dpp<XOR2>(M));
        const float e = __builtin_amdgcn_exp2f(ms - M);
        float Lt = e * ls; Lt += dpp<XOR1>(Lt); Lt += dpp<XOR2>(Lt);
        const int wgt = __builtin_bit_cast(int, e / Lt);
        const int f0 = __builtin_amdgcn_readlane(wgt, s), f1 = __builtin_amdgcn_readlane(wgt, 4 + s);
        oscale = __builtin_bit_cast(float, head ? f1 : f0);
      }
      v4u pf = *(const LAS v4u*)(L + pa + t * 128);
      if (t == NKT - 1) tgate = RFL(ctl[13]) + ((NT * TAU) >> 4) + G1;
      wait_full(NKT + t + 1);
      LAS unsigned char* tb = L + (t & (NSLOT - 1)) * TILE_B;
      v4u av[4];
#pragma unroll
      for (int u = 0; u < 4; ++u) { const v2u lo = trread(tb + va[u]), hi = trread(tb + va[u] + 8192); av[u] = v4u{lo.x, lo.y, hi.x, hi.y}; }
      LDSW();
      ctl[4 + w] = (unsigned)(NKT + t + 1);
      asm volatile("" : "+v"(av[0]), "+v"(av[1]), "+v"(av[2]), "+v"(av[3]), "+v"(pf) :: "memory");
#pragma unroll
      for (int u = 0; u < 4; ++u) acc[u] = mfma(av[u], pf, acc[u]);
    }
  }
  STAMP(5 + w);
  {
    const int u = (r16 >> 1) & 3;
    const f32x4 a01 = (u & 1) ? acc[1] : acc[0], a23 = (u & 1) ? acc[3] : acc[2];
    const f32x4 a = ((u & 2) ? a23 : a01) * oscale;
    const v4u o = {cvtpk(a[0], a[1]), tag, cvtpk(a[2], a[3]), tag};
    const int off = WS_PO + ((b * 16 + kvh * 2 + head) * 4 + s) * 1024 + (64 * w + 16 * u + 4 * g) * 4;
    ST_SC1(o, (r16 < 8 && live) ? off : WS_BYTES);
  }
  if (!has_out) { STAMP_FLUSH(); return; }
  const u64 wsa = (u64)ws;
  const v4i srd = {(int)(unsigned)wsa, (int)((unsigned)(wsa >> 32) & 0xffffu), WS_BYTES, 0x00020000};
  v4u p0, p1, p2, p3;
  auto gather_issue = [&](int voff) {
    asm volatile("buffer_load_dwordx4 %0, %4, %5, 0 offen sc1\n\t"
                 "buffer_load_dwordx4 %1, %4, %5, 0 offen offset:1024 sc1\n\t"
                 "buffer_load_dwordx4 %2, %4, %5, 0 offen offset:2048 sc1\n\t"
                 "buffer_load_dwordx4 %3, %4, %5, 0 offen offset:3072 sc1"
                 : "=&v"(p0), "=&v"(p1), "=&v"(p2), "=&v"(p3) : "v"(voff), "s"(srd) : "memory");
  };
  auto gather_wait = [&](int voff) {
    int sp = 0;
    WAIT(true, 16) {
      asm volatile("s_waitcnt vmcnt(0)" : "+v"(p0), "+v"(p1), "+v"(p2), "+v"(p3) :: "memory");
      const unsigned want = tag;
      const u64 m0 = __builtin_amdgcn_ballot_w64(p0.y != want || p0.w != want), m1 = __builtin_amdgcn_ballot_w64(p1.y != want || p1.w != want);
      const u64 m2 = __builtin_amdgcn_ballot_w64(p2.y != want || p2.w != want), m3 = __builtin_amdgcn_ballot_w64(p3.y != want || p3.w != want);
      if (!(m0 | m1 | m2 | m3)) break;
      if (++sp > 16) __builtin_amdgcn_s_sleep(8);
      asm volatile("s_mov_b64 exec, %5\n\tbuffer_load_dwordx4 %0, %4, %9, 0 offen sc1\n\t"
                   "s_mov_b64 exec, %6\n\tbuffer_load_dwordx4 %1, %4, %9, 0 offen offset:1024 sc1\n\t"
                   "s_mov_b64 exec, %7\n\tbuffer_load_dwordx4 %2, %4, %9, 0 offen offset:2048 sc1\n\t"
                   "s_mov_b64 exec, %8\n\tbuffer_load_dwordx4 %3, %4, %9, 0 offen offset:3072 sc1\n\t"
                   "s_mov_b64 exec, -1"
                   : "+v"(p0), "+v"(p1), "+v"(p2), "+v"(p3) : "v"(voff), "s"(m0), "s"(m1), "s"(m2), "s"(m3), "s"(srd) : "memory");
    }
    asm volatile("s_waitcnt vmcnt(0)" : "+v"(p0), "+v"(p1), "+v"(p2), "+v"(p3) :: "memory");
  };
  {
    const int po = WS_PO + ((lane >> 3) * 16 + (rg >> 1)) * 4096 + ((rg & 1) * 128 + 32 * w) * 4 + (lane & 7) * 16;
    SPIN sp < 32 && (int)(now() - tgate) < 0; ++sp) __builtin_amdgcn_s_sleep(1);
    gather_issue(po);
    WAIT(RFL(ctl[11]) == 0u, 1) { __builtin_amdgcn_s_sleep(1); }
    asm volatile("" ::: "memory");
    v4u wa[4];
    {
      LAS unsigned char* wb = L + S_OFF + (8 * g + qq) * 128 + w * 32 + pp * 8;
#pragma unroll
      for (int k = 0; k < 4; ++k) { const v2u lo = trread(wb + k * 4096), hi = trread(wb + k * 4096 + 512); wa[k] = v4u{lo.x, lo.y, hi.x, hi.y}; }
    }
    const int yo = WS_Y2 + ((((g & 1) * 4 + (g >> 1)) * 7 + lc) * 4 + w) * 4096 + rg * 128 + r16 * 8;
    gather_wait(po);
    STAMP(9 + w);
    LAS unsigned char* xb = L + X_OFF + w * 2048 + lane * 8;
    *(LAS v2u*)(xb) = v2u{p0.x, p0.z}; *(LAS v2u*)(xb + 512) = v2u{p1.x, p1.z};
    *(LAS v2u*)(xb + 1024) = v2u{p2.x, p2.z}; *(LAS v2u*)(xb + 1536) = v2u{p3.x, p3.z};
    LDSW();
    __builtin_amdgcn_s_barrier();
    asm volatile("" ::: "memory");
    v4u oa[8];
#pragma unroll
    for (int k = 0; k < 8; ++k) oa[k] = *(const LAS v4u*)(L + X_OFF + k * 1024 + r16 * 64 + g * 16);
    f32x4 ya = {0.f, 0.f, 0.f, 0.f}, yb = {0.f, 0.f, 0.f, 0.f};
#pragma unroll
    for (int k = 0; k < 4; ++k) { ya = mfma(oa[2 * k], wa[k], ya); yb = mfma(oa[2 * k + 1], wa[k], yb); }
    ya += yb;
    auto t0 = __builtin_amdgcn_permlane32_swap(__float_as_uint(ya[0]), __float_as_uint(ya[1]), false, false);
    auto t1 = __builtin_amdgcn_permlane32_swap(__float_as_uint(ya[2]), __float_as_uint(ya[3]), false, false);
    const float v0 = __uint_as_float(t0[0]) + __uint_as_float(t0[1]), v1 = __uint_as_float(t1[0]) + __uint_as_float(t1[1]);
    __builtin_amdgcn_raw_buffer_store_b64(v2u{__float_as_uint(v0), tag}, rs, yo, 0, 16);
    __builtin_amdgcn_raw_buffer_store_b64(v2u{__float_as_uint(v1), tag}, rs, yo + 2 * 7 * 4 * 4096, 0, 16);
  }
  if (w != 0) { STAMP_FLUSH(); return; }
  STAMP(14);
  {
    const int nt = (bid >> 3) & 3;
    const int yo = WS_Y2 + ((b3 * 7 + lc) * 4 + nt) * 4096 + lane * 16;
    __builtin_amdgcn_s_sleep(D2);
    gather_issue(yo);
    gather_wait(yo);
    STAMP(15);
    float v0 = (__uint_as_float(p0.x) + __uint_as_float(p1.x)) + (__uint_as_float(p2.x) + __uint_as_float(p3.x));
    float v1 = (__uint_as_float(p0.z) + __uint_as_float(p1.z)) + (__uint_as_float(p2.z) + __uint_as_float(p3.z));
    v0 += dpp<XOR8>(v0); v1 += dpp<XOR8>(v1);
    v0 = swap32add(swap16add(v0)); v1 = swap32add(swap16add(v1));
    const __amdgpu_buffer_rsrc_t ro = __builtin_amdgcn_make_buffer_rsrc((void*)out, 0, B * D * 2, 0x00020000);
    __builtin_amdgcn_raw_buffer_store_b32(cvtpk(v0, v1), ro, (lane < 8 && live) ? (b3 * D + lc * 64 + nt * 16 + 2 * lane) * 2 : B * D * 2, 0, 0);
    __builtin_amdgcn_raw_buffer_store_b32(tag, rs, (bid == 0 && lane == 0 && live) ? WS_E : WS_BYTES, 0, 16);
    STAMP(13);
    STAMP_FLUSH();
  }
}

extern "C" void kernel_launch(void* const* d_in, const int* in_sizes, int n_in, void* d_out, int out_size, void* d_ws, size_t ws_size, hipStream_t stream) {
  (void)hipFuncSetAttribute((const void*)k_fused, hipFuncAttributeMaxDynamicSharedMemorySize, LDS_BYTES);
  k_fused<<<B * HKV * NSPLIT, NWAVE * 64, LDS_BYTES, stream>>>((const unsigned short*)d_in[0], (const unsigned short*)d_in[1], (const unsigned short*)d_in[2],
      (const unsigned short*)d_in[3], (const unsigned short*)d_in[4], (unsigned short*)d_out, (unsigned char*)d_ws);
}
```

```cpp
#include <hip/hip_runtime.h>
#include <hip/hip_bf16.h>
#include <stdint.h>

typedef unsigned v4u __attribute__((ext_vector_type(4)));
typedef unsigned v2u __attribute__((ext_vector_type(2)));
typedef int v4i __attribute__((ext_vector_type(4)));
typedef float f32x4 __attribute__((ext_vector_type(4)));
typedef float f32x2 __attribute__((ext_vector_type(2)));
typedef short s16x4 __attribute__((ext_vector_type(4)));
typedef __bf16 bf16x8 __attribute__((ext_vector_type(8)));
typedef __bf16 bf16x2 __attribute__((ext_vector_type(2)));
typedef unsigned long long u64;
#define LAS __attribute__((address_space(3)))
#define GAS __attribute__((address_space(1)))
#define RLX_AGENT __ATOMIC_RELAXED, __HIP_MEMORY_SCOPE_AGENT

constexpr int B = 8, D = 448, HKV = 8, SK = 8192, DH = 256;
constexpr int NSPLIT = 4;
constexpr int KPS = SK / NSPLIT;
#define TAU 888
#define NPRE 3
#define ST_AT 40
#define D1 0
#define D2 16
#define TH1 25
#define TH2 120
#define LEAD 30
#define PK0 8
#define PKG 24
#define PKD 3
#define QD 550
#define WO_AT 126
#define W0 16
#define ENDW 0
#define ENDV 1000
#define G1 150
#define G2 0
#define MAP2 1
#define XL 1
#define OT 0
#define OTA 116
#define OSC 0
#define MIXA 4
#define MIXB 5
#define MIXO 0
#define S3W 1
#define S3D 4
#define WQ_AUX 2
constexpr int TILE_B = 16384, NSLOT = 8, NKT = KPS / 32, NT = 2 * NKT;
constexpr int NCONS = 4, NWAVE = 6;
constexpr int WAIT_BUDGET = 1 << 22;
constexpr int S_OFF = NSLOT * TILE_B;
constexpr int X_OFF = S_OFF + 2 * KPS * 4;
constexpr int P_OFF = X_OFF + 2048;
constexpr int Q_OFF = P_OFF + NKT * 128;
constexpr int C_OFF = Q_OFF + 1024;
constexpr int LDS_BYTES = C_OFF + 256;
constexpr int WS_E = 0;
constexpr int WS_QG = 4096;
constexpr int WS_ST = WS_QG + 8 * 2048 * 8;
constexpr int WS_PO = WS_ST + 8 * 16 * 4 * 16;
constexpr int WS_Y2 = WS_PO + 8 * 16 * 4 * 1024;
constexpr int WS_PH = WS_Y2 + 8 * 7 * 4 * 4096;
constexpr int WS_BYTES = WS_PH + 256 * 128;

__device__ __forceinline__ unsigned cvtpk(float lo, float hi) { f32x2 v = {lo, hi}; return __builtin_bit_cast(unsigned, __builtin_convertvector(v, bf16x2)); }
__device__ __forceinline__ f32x4 mfma(v4u a, v4u b, f32x4 c) {
  return __builtin_amdgcn_mfma_f32_16x16x32_bf16(__builtin_bit_cast(bf16x8, a), __builtin_bit_cast(bf16x8, b), c, 0, 0, 0);
}
__device__ __forceinline__ v2u trread(LAS unsigned char* p) { return __builtin_bit_cast(v2u, __builtin_amdgcn_ds_read_tr16_b64_v4i16((LAS s16x4*)p)); }
__device__ __forceinline__ float x16max(float x) {
  auto s = __builtin_amdgcn_permlane16_swap(__float_as_uint(x), __float_as_uint(x), false, false);
  x = fmaxf(__uint_as_float(s[0]), __uint_as_float(s[1]));
  auto t = __builtin_amdgcn_permlane32_swap(__float_as_uint(x), __float_as_uint(x), false, false);
  return fmaxf(__uint_as_float(t[0]), __uint_as_float(t[1]));
}
template <int CTRL> __device__ __forceinline__ float dpp(float x) {
  return __builtin_bit_cast(float, __builtin_amdgcn_mov_dpp(__builtin_bit_cast(int, x), CTRL, 0xf, 0xf, true));
}
constexpr int XOR1 = 0xB1, XOR2 = 0x4E, XOR7 = 0x141, XOR8 = 0x128;
__device__ __forceinline__ float swap32add(float a) {
  auto t = __builtin_amdgcn_permlane32_swap(__float_as_uint(a), __float_as_uint(a), false, false);
  return __uint_as_float(t[0]) + __uint_as_float(t[1]);
}
__device__ __forceinline__ float swap16add(float a) {
  auto t = __builtin_amdgcn_permlane16_swap(__float_as_uint(a), __float_as_uint(a), false, false);
  return __uint_as_float(t[0]) + __uint_as_float(t[1]);
}
__device__ __forceinline__ float wavesum(float x) {
  x += dpp<XOR1>(x); x += dpp<XOR2>(x); x += dpp<XOR7>(x); x += dpp<XOR8>(x);
  return swap32add(swap16add(x));
}
__device__ __forceinline__ unsigned now() { return (unsigned)__builtin_amdgcn_s_memrealtime(); }
#define LDSW() asm volatile("s_waitcnt lgkmcnt(0)" ::: "memory")
#define DRAIN() asm volatile("s_waitcnt vmcnt(0)" ::: "memory")
#define PIN16(a) asm volatile("" : "+v"(a[0]), "+v"(a[1]), "+v"(a[2]), "+v"(a[3]), "+v"(a[4]), "+v"(a[5]), "+v"(a[6]), "+v"(a[7]), \
                        "+v"(a[8]), "+v"(a[9]), "+v"(a[10]), "+v"(a[11]), "+v"(a[12]), "+v"(a[13]), "+v"(a[14]), "+v"(a[15]) :: "memory")
#define RFL(x) __builtin_amdgcn_readfirstlane(x)
#define LD_SC1(off) __builtin_amdgcn_raw_buffer_load_b128(rs, (off), 0, 16)
#define ST_SC1(v, off) __builtin_amdgcn_raw_buffer_store_b128((v), rs, (off), 0, 16)
#define SPIN _Pragma("nounroll") for (int sp = 0;
#define WAIT(cond, cost) _Pragma("nounroll") for (; bud > 0 && (cond); bud -= (cost))

__global__ void __launch_bounds__(NWAVE * 64, 1) k_fused(const unsigned short* __restrict__ x, const unsigned short* __restrict__ kc, const unsigned short* __restrict__ vc,
                                              const unsigned short* __restrict__ wq, const unsigned short* __restrict__ wo, unsigned short* __restrict__ out, unsigned char* ws) {
  extern __shared__ __attribute__((aligned(16))) unsigned char lds[];
  LAS unsigned char* L = (LAS unsigned char*)lds;
  volatile LAS unsigned* ctl = (volatile LAS unsigned*)(L + C_OFF);
  const int bid = blockIdx.x;
  const int lane = threadIdx.x & 63, wave = RFL(threadIdx.x >> 6);
  int bud = WAIT_BUDGET;
#define STAMP(i)
#define STAMP_FLUSH()
  unsigned o0 = (lane >> 5) * 512 + (((lane & 31) ^ ((lane >> 5) << 1)) << 4);
  unsigned o1 = o0 ^ 64, o2 = o0 ^ 128, o3 = o0 ^ 192;
  const unsigned ldsbase = (unsigned)(size_t)L;
  u64 src = (u64)kc + (u64)bid * (KPS * DH * 2);
#define DMA4(S) "global_load_lds_dwordx4 %1, " S " nt\n\tglobal_load_lds_dwordx4 %2, " S " offset:1024 nt\n\t" \
                "global_load_lds_dwordx4 %3, " S " offset:2048 nt\n\tglobal_load_lds_dwordx4 %4, " S " offset:3072 nt\n\t"
  {
    unsigned keep, cnt, c0 = o0, c1 = o1, c2 = o2, c3 = o3;
    asm volatile("s_cmp_lg_u32 %8, 0\n\ts_cbranch_scc1 .Lskip%=\n\t"
                 "s_setprio 3\n\t"
                 "s_mov_b32 %0, m0\n\ts_mov_b32 m0, %7\n\ts_mov_b32 %5, 8\n"
                 ".Lloop%=:\n\t" DMA4("%6")
                 "s_add_u32 m0, m0, 0x1000\n\t"
                 "v_add_u32 %1, 0x1000, %1\n\tv_add_u32 %2, 0x1000, %2\n\tv_add_u32 %3, 0x1000, %3\n\tv_add_u32 %4, 0x1000, %4\n\t"
                 "s_sub_u32 %5, %5, 1\n\ts_cmp_lg_u32 %5, 0\n\ts_cbranch_scc1 .Lloop%=\n\t"
                 "s_mov_b32 m0, %0\n"
                 ".Lskip%=:"
                 : "=&s"(keep), "+v"(c0), "+v"(c1), "+v"(c2), "+v"(c3), "=&s"(cnt)
                 : "s"(src), "s"(ldsbase), "s"(wave) : "memory", "scc");
  }
  if (threadIdx.x < 64) ((LAS unsigned*)(L + C_OFF))[lane] = 0u;
  LDSW();
  __builtin_amdgcn_s_barrier();
  if (wave == 0) {
    const u64 vb = (u64)vc + (u64)bid * (KPS * DH * 2);
    src += 2 * TILE_B;
    int pub = 0;
    unsigned phi = 0;
#pragma nounroll
    for (int t = 2; t < NT + 3; ++t) {
      if (t >= NSLOT && t < NT) {
        const unsigned needt = (unsigned)(t - NSLOT);
        WAIT(true, 1) {
          const v4u nd = *(volatile LAS v4u*)(L + C_OFF + 16);
          const unsigned mn = min(min(nd.x, nd.y), min(nd.z, nd.w));
          if (RFL(mn) > needt) break;
          __builtin_amdgcn_s_sleep(1);
        }
        asm volatile("" ::: "memory");
      }
      const v2u rf = *(volatile LAS v2u*)(L + C_OFF + 48);
      const unsigned rvalid = RFL(rf.x), ref = RFL(rf.y);
      const int e = t >= NT ? (t == NT ? TH2 + 1 : t == NT + 1 ? TH1 + 1 : 0) : t >= NT - ENDW ? ENDV : rvalid ? (int)(phi - ref) : 0;
      const unsigned tt = ((unsigned)t * TAU) >> 4;
      const int issued = t < NT ? t : NT;
      if (t == NKT) src = vb;
      int landed; unsigned adj;
      if (e > TH2) { asm volatile("s_waitcnt vmcnt(47)" ::: "memory"); landed = issued - 3; adj = ((47 - W0) * TAU) >> 8; }
      else if (e > TH1) { asm volatile("s_waitcnt vmcnt(32)" ::: "memory"); landed = issued - 2; adj = ((32 - W0) * TAU) >> 8; }
      else if (W0 > 16 && t > NT + 1) { asm volatile("s_waitcnt vmcnt(16)" ::: "memory"); landed = issued - 1; adj = 0; }
      else {
        asm volatile("s_waitcnt vmcnt(%0)" :: "i"(W0) : "memory"); landed = issued - (W0 > 16 ? 2 : 1); adj = 0;
        if (e < 0) { const unsigned due = ref + tt - LEAD; SPIN sp < 64 && (int)(now() - due) < 0; ++sp) __builtin_amdgcn_s_sleep(1); }
      }
      if (t < NT) {
        const unsigned dst = ldsbase + (t & (NSLOT - 1)) * TILE_B;
        const u64 s1 = src + 4096, s2 = src + 8192, s3 = src + 12288;
        unsigned keep;
        asm volatile("s_mov_b32 %0, m0\n\t"
                     "s_mov_b32 m0, %9\n\ts_nop 0\n\t" DMA4("%5")
                     "s_add_u32 m0, %9, 0x1000\n\ts_nop 0\n\t" DMA4("%6")
                     "s_add_u32 m0, %9, 0x2000\n\ts_nop 0\n\t" DMA4("%7")
                     "s_add_u32 m0, %9, 0x3000\n\ts_nop 0\n\t" DMA4("%8")
                     "s_mov_b32 m0, %0"
                     : "=&s"(keep) : "v"(o0), "v"(o1), "v"(o2), "v"(o3), "s"(src), "s"(s1), "s"(s2), "s"(s3), "s"(dst) : "memory", "scc");
        src += TILE_B;
      }
      if (landed > pub) pub = landed;
      phi = now() - tt + adj;
      *(volatile LAS v2u*)(L + C_OFF) = v2u{(unsigned)pub, phi};
    }
    DRAIN();
    ctl[0] = (unsigned)NT;
    return;
  }
  const int s = bid & 3, kvh = (bid >> 2) & 7, b = bid >> 5;
  const bool has_out = (bid >> 5) < 7;
  const int lc = has_out ? (bid >> 5) : 0, rg = (bid & 7) * 4 + ((bid >> 3) & 3);
  const int b3 = bid & 7;
  auto wait_full = [&](unsigned n) {
    WAIT(RFL(ctl[0]) < n, 1) { __builtin_amdgcn_s_sleep(1); }
    asm volatile("" ::: "memory");
  };
  auto wait_full_slow = [&](unsigned n) {
    WAIT(RFL(ctl[0]) < n, 4) { __builtin_amdgcn_s_sleep(4); }
    asm volatile("" ::: "memory");
  };
  if (wave == 5) {
    WAIT(RFL(ctl[2]) == 0u, 4) { __builtin_amdgcn_s_sleep(4); }
    const unsigned tag = RFL(ctl[3]);
    int mixs = MIXA;
#pragma nounroll
    for (int k = PK0; k + PKD < WO_AT; k += PKG) {
      wait_full_slow(k);
      const unsigned mine = RFL(ctl[1]);
      if (lane == 0) __hip_atomic_store((GAS u64*)(ws + WS_PH) + bid * 16, ((u64)tag << 32) | mine, RLX_AGENT);
      wait_full_slow(k + PKD);
      u64 o = 0;
      if (lane < 8) o = __hip_atomic_load((GAS u64*)(ws + WS_PH) + (((((bid >> 3) + mixs * (lane + MIXO)) & 31) << 3) + lane) * 16, RLX_AGENT);
      mixs = MIXB - mixs;
      const bool ok = (unsigned)(o >> 32) == tag && lane < 8;
      const float dsum = wavesum(ok ? (float)(int)((unsigned)o - mine) : 0.f);
      const int n = __builtin_popcountll(__builtin_amdgcn_ballot_w64(ok));
      if (n >= 4) { const unsigned ref = mine + (unsigned)(int)(dsum / (float)n); *(volatile LAS v2u*)(L + C_OFF + 48) = v2u{1u, ref}; }
    }
    if (!has_out) return;
    const unsigned char* src = (const unsigned char*)wo + (size_t)(rg * 128 + (lane >> 3)) * (D * 2) + lc * 128 + (lane & 7) * 16;
    wait_full_slow(WO_AT);
    WAIT(RFL(ctl[9]) < NCONS, 4) { __builtin_amdgcn_s_sleep(4); }
    asm volatile("" ::: "memory");
#pragma unroll
    for (int m = 0; m < 16; ++m) __builtin_amdgcn_global_load_lds((const GAS unsigned*)(src + m * 8 * (D * 2)), (LAS unsigned*)(L + S_OFF + m * 1024), 16, 0, 2);
    DRAIN();
    ctl[11] = 1u;
    return;
  }
  const __amdgpu_buffer_rsrc_t rs = __builtin_amdgcn_make_buffer_rsrc((void*)ws, 0, WS_BYTES, 0x00020000);
  const bool live = true;
  const int w = wave - 1;
  const int g = lane >> 4, r16 = lane & 15, head = lane & 1;
  const int qq = (lane >> 2) & 3, pp = lane & 3;
  unsigned tag = 0;
  f32x4 acc[4];
#pragma unroll
  for (int u = 0; u < 4; ++u) acc[u] = f32x4{0.f, 0.f, 0.f, 0.f};
  float oscale = 0.f;
  unsigned tgate = 0;
  {
    unsigned ka[4];
#pragma unroll
    for (int i = 0; i < 4; ++i) ka[i] = r16 * 512 + (((4 * i + g) ^ (2 * (r16 & 7))) << 4);
    v4u pre[NPRE][16];
    if (w == 0) {
      const unsigned tq0 = now();
      ctl[4] = (unsigned)(3 * NPRE);
      unsigned eraw = __hip_atomic_load((GAS unsigned*)(ws + WS_E), RLX_AGENT);
      const int ctile = ((bid & 7) + 8 * (bid >> 5)) * 4 + ((bid >> 3) & 3);
      const unsigned short* wsrc = wq + (size_t)(lane >> 1) * 4096 + ctile * 16 + (lane & 1) * 8;
#pragma unroll
      for (int j = 0; j < 14; ++j) __builtin_amdgcn_global_load_lds((const GAS unsigned*)(wsrc + (size_t)j * 32 * 4096), (LAS unsigned*)(L + S_OFF + j * 1024), 16, 0, WQ_AUX);
      v4u xf[14];
#pragma unroll
      for (int j = 0; j < 7; ++j) __builtin_amdgcn_global_load_lds((const GAS unsigned*)(x + j * 512 + lane * 8), (LAS unsigned*)(L + X_OFF + j * 1024), 16, 0, 0);
      __builtin_amdgcn_sched_barrier(0);
      DRAIN();
#pragma unroll
      for (int j = 0; j < 14; ++j) xf[j] = *(const LAS v4u*)(L + X_OFF + (lane & 7) * (D * 2) + 64 * j + 16 * g);
      asm volatile("" : "+v"(eraw));
      tag = RFL(eraw) + 1u;
      *(volatile LAS v2u*)(L + C_OFF + 8) = v2u{1u, tag};
      f32x4 d = {0.f, 0.f, 0.f, 0.f}, d2 = {0.f, 0.f, 0.f, 0.f};
      LAS unsigned char* wb = L + S_OFF + (8 * g + qq) * 32 + pp * 8;
      v4u wf[14];
#pragma unroll
      for (int j = 0; j < 14; ++j) { const v2u lo = trread(wb + j * 1024), hi = trread(wb + j * 1024 + 128); wf[j] = v4u{lo.x, lo.y, hi.x, hi.y}; }
      __builtin_amdgcn_sched_barrier(0);
#pragma unroll
      for (int j = 0; j < 14; j += 2) { d = mfma(wf[j], xf[j], d); d2 = mfma(wf[j + 1], xf[j + 1], d2); }
      d += d2;
      if (r16 < 8) {
        const v4u gr = {cvtpk(d[0], d[1]), tag, cvtpk(d[2], d[3]), tag};
        ST_SC1(gr, WS_QG + (r16 * 2048 + ctile * 8 + 2 * g) * 8);
      }
      STAMP(3);
      const int qo = WS_QG + (b * 2048 + kvh * 256) * 8 + lane * 16;
      v4u ga = {0u, 0u, 0u, 0u}, gb = ga;
      SPIN sp < 1024 && (int)(now() - tq0) < QD; ++sp) __builtin_amdgcn_s_sleep(2);
      WAIT(true, 16) {
        ga = LD_SC1(qo);
        gb = LD_SC1(qo + 1024);
        const bool ok = ga.y == tag && ga.w == tag && gb.y == tag && gb.w == tag;
        if (__all(ok)) break;
        __builtin_amdgcn_s_sleep(8);
        asm volatile("" ::: "memory");
      }
      *(LAS v2u*)(L + Q_OFF + lane * 8) = v2u{ga.x, ga.z};
      *(LAS v2u*)(L + Q_OFF + 512 + lane * 8) = v2u{gb.x, gb.z};
      LDSW();
      ctl[10] = 1u;
      STAMP(4);
    } else {
#pragma unroll
      for (int j = 0; j < NPRE; ++j) {
        const int t = (w - 1) + 3 * j;
        wait_full(t + 1);
        LAS unsigned char* tb = L + (t & (NSLOT - 1)) * TILE_B;
#pragma unroll
        for (int i = 0; i < 8; ++i) { pre[j][i] = *(const LAS v4u*)(tb + ka[i & 3] + (i >> 2) * 256); pre[j][8 + i] = *(const LAS v4u*)(tb + ka[i & 3] + (i >> 2) * 256 + 8192); }
        LDSW();
        ctl[4 + w] = (unsigned)(j + 1 < NPRE ? t + 3 : 3 * NPRE + w);
        PIN16(pre[j]);
      }
      WAIT(RFL(ctl[10]) == 0u, 2) { __builtin_amdgcn_s_sleep(2); }
      asm volatile("" ::: "memory");
      tag = RFL(ctl[3]);
    }
    v4u qf[8];
#pragma unroll
    for (int i = 0; i < 8; ++i) qf[i] = *(const LAS v4u*)(L + Q_OFF + head * 512 + 64 * i + 16 * g);
    float mx = -INFINITY;
    auto qk = [&](const v4u (&a)[16], int t) {
      f32x4 d0 = {0.f, 0.f, 0.f, 0.f}, d1 = {0.f, 0.f, 0.f, 0.f};
#pragma unroll
      for (int i = 0; i < 8; ++i) { d0 = mfma(a[i], qf[i], d0); d1 = mfma(a[8 + i], qf[i], d1); }
      mx = fmaxf(mx, fmaxf(fmaxf(fmaxf(d0[0], d0[1]), fmaxf(d0[2], d0[3])), fmaxf(fmaxf(d1[0], d1[1]), fmaxf(d1[2], d1[3]))));
      if (r16 < 4) {
        const f32x4 d = (r16 & 2) ? d1 : d0;
        *(LAS f32x4*)(L + S_OFF + head * (KPS * 4) + (t * 32 + (r16 & 2) * 8 + 4 * g) * 4) = d;
      }
    };
    if (w > 0) {
#pragma unroll
      for (int j = 0; j < NPRE; ++j) qk(pre[j], (w - 1) + 3 * j);
    }
#pragma nounroll
    for (int t = 3 * NPRE + w; t < NKT; t += NCONS) {
      wait_full(t + 1);
      LAS unsigned char* tb = L + (t & (NSLOT - 1)) * TILE_B;
      v4u a[16];
#pragma unroll
      for (int i = 0; i < 8; ++i) { a[i] = *(const LAS v4u*)(tb + ka[i & 3] + (i >> 2) * 256); a[8 + i] = *(const LAS v4u*)(tb + ka[i & 3] + (i >> 2) * 256 + 8192); }
      LDSW();
      ctl[4 + w] = (unsigned)(t + NCONS < NKT ? t + NCONS : NKT);
      PIN16(a);
      qk(a, t);
    }
    mx = x16max(mx);
    if (lane < 2) ((LAS float*)(L + C_OFF))[16 + w * 2 + lane] = mx;
    LDSW();
    if (lane == 0) __hip_atomic_fetch_add((LAS unsigned*)(L + C_OFF) + 8, 1u, __ATOMIC_RELAXED, __HIP_MEMORY_SCOPE_WORKGROUP);
    WAIT(RFL(ctl[8]) < NCONS, 1) { __builtin_amdgcn_s_sleep(1); }
    asm volatile("" ::: "memory");
    constexpr float C2 = 0.0625f * 1.4426950408889634f;
    {
      const f32x4 ma = *(const LAS f32x4*)(L + C_OFF + 64), mb = *(const LAS f32x4*)(L + C_OFF + 80);
      const float M0 = fmaxf(fmaxf(ma[0], ma[2]), fmaxf(mb[0], mb[2])) * C2, M1 = fmaxf(fmaxf(ma[1], ma[3]), fmaxf(mb[1], mb[3])) * C2;
      const int tt = w * 64 + lane, tile = tt >> 2, gg = tt & 3;
      float sum[2];
#pragma unroll
      for (int h = 0; h < 2; ++h) {
        const f32x4 sa = *(const LAS f32x4*)(L + S_OFF + h * (KPS * 4) + (tile * 32 + 4 * gg) * 4);
        const f32x4 sb = *(const LAS f32x4*)(L + S_OFF + h * (KPS * 4) + (tile * 32 + 16 + 4 * gg) * 4);
        const float mc = h ? M1 : M0;
        float p[8];
#pragma unroll
        for (int j = 0; j < 4; ++j) { p[j] = __builtin_amdgcn_exp2f(fmaf(sa[j], C2, -mc)); p[4 + j] = __builtin_amdgcn_exp2f(fmaf(sb[j], C2, -mc)); }
        sum[h] = ((p[0] + p[1]) + (p[2] + p[3])) + ((p[4] + p[5]) + (p[6] + p[7]));
        v4u pk = {cvtpk(p[0], p[1]), cvtpk(p[2], p[3]), cvtpk(p[4], p[5]), cvtpk(p[6], p[7])};
        *(LAS v4u*)(L + P_OFF + tile * 128 + gg * 32 + h * 16) = pk;
      }
      sum[0] = wavesum(sum[0]); sum[1] = wavesum(sum[1]);
      if (lane < 2) ((LAS float*)(L + C_OFF))[24 + w * 2 + lane] = lane ? sum[1] : sum[0];
      LDSW();
      if (lane == 0) __hip_atomic_fetch_add((LAS unsigned*)(L + C_OFF) + 9, 1u, __ATOMIC_RELAXED, __HIP_MEMORY_SCOPE_WORKGROUP);
      WAIT(RFL(ctl[9]) < NCONS, 1) { __builtin_amdgcn_s_sleep(1); }
      asm volatile("" ::: "memory");
      if (w == 1 && lane < 2) {
        const f32x4 la = *(const LAS f32x4*)(L + C_OFF + 96), lb = *(const LAS f32x4*)(L + C_OFF + 112);
        const float Ls = lane ? ((la[1] + la[3]) + (lb[1] + lb[3])) : ((la[0] + la[2]) + (lb[0] + lb[2]));
        const v4u gr = {__float_as_uint(lane ? M1 : M0), tag, __float_as_uint(Ls), tag};
        ST_SC1(gr, WS_ST + ((b * 16 + kvh * 2 + lane) * 4 + s) * 16);
      }
    }
    const int key = 4 * g + qq;
    unsigned va[4];
#pragma unroll
    for (int u = 0; u < 4; ++u) va[u] = key * 512 + (((8 * w + 2 * u + (pp >> 1)) ^ (2 * (key & 7))) << 4) + (pp & 1) * 8;
    const unsigned pa = P_OFF + g * 32 + head * 16;
#pragma nounroll
    for (int t = 0; t < NKT; ++t) {
      if (t == ST_AT) {
        v4u sc = {0u, 0u, 0u, 0u};
        WAIT(true, 16) {
          sc = LD_SC1(WS_ST + (b * 16 + kvh * 2) * 64 + (lane & 7) * 16);
          if (__all(sc.y == tag && sc.w == tag)) break;
          __builtin_amdgcn_s_sleep(8);
          asm volatile("" ::: "memory");
        }
        const float ms = __uint_as_float(sc.x), ls = __uint_as_float(sc.z);
        float M = fmaxf(ms, dpp<XOR1>(ms)); M = fmaxf(M, dpp<XOR2>(M));
        const float e = __builtin_amdgcn_exp2f(ms - M);
        float Lt = e * ls; Lt += dpp<XOR1>(Lt); Lt += dpp<XOR2>(Lt);
        const int wgt = __builtin_bit_cast(int, e / Lt);
        const int f0 = __builtin_amdgcn_readlane(wgt, s), f1 = __builtin_amdgcn_readlane(wgt, 4 + s);
        oscale = __builtin_bit_cast(float, head ? f1 : f0);
      }
      v4u pf = *(const LAS v4u*)(L + pa + t * 128);
      if (t == NKT - 1) tgate = RFL(ctl[13]) + ((NT * TAU) >> 4) + G1;
      wait_full(NKT + t + 1);
      LAS unsigned char* tb = L + (t & (NSLOT - 1)) * TILE_B;
      v4u av[4];
#pragma unroll
      for (int u = 0; u < 4; ++u) { const v2u lo = trread(tb + va[u]), hi = trread(tb + va[u] + 8192); av[u] = v4u{lo.x, lo.y, hi.x, hi.y}; }
      LDSW();
      ctl[4 + w] = (unsigned)(NKT + t + 1);
      asm volatile("" : "+v"(av[0]), "+v"(av[1]), "+v"(av[2]), "+v"(av[3]), "+v"(pf) :: "memory");
#pragma unroll
      for (int u = 0; u < 4; ++u) acc[u] = mfma(av[u], pf, acc[u]);
    }
  }
  STAMP(5 + w);
  {
    const int u = (r16 >> 1) & 3;
    const f32x4 a01 = (u & 1) ? acc[1] : acc[0], a23 = (u & 1) ? acc[3] : acc[2];
    const f32x4 a = ((u & 2) ? a23 : a01) * oscale;
    const v4u o = {cvtpk(a[0], a[1]), tag, cvtpk(a[2], a[3]), tag};
    const int off = WS_PO + ((b * 16 + kvh * 2 + head) * 4 + s) * 1024 + (64 * w + 16 * u + 4 * g) * 4;
    ST_SC1(o, (r16 < 8 && live) ? off : WS_BYTES);
  }
  if (!has_out) { STAMP_FLUSH(); return; }
  const u64 wsa = (u64)ws;
  const v4i srd = {(int)(unsigned)wsa, (int)((unsigned)(wsa >> 32) & 0xffffu), WS_BYTES, 0x00020000};
  v4u p0, p1, p2, p3;
  auto gather_issue = [&](int voff) {
    asm volatile("buffer_load_dwordx4 %0, %4, %5, 0 offen sc1\n\t"
                 "buffer_load_dwordx4 %1, %4, %5, 0 offen offset:1024 sc1\n\t"
                 "buffer_load_dwordx4 %2, %4, %5, 0 offen offset:2048 sc1\n\t"
                 "buffer_load_dwordx4 %3, %4, %5, 0 offen offset:3072 sc1"
                 : "=&v"(p0), "=&v"(p1), "=&v"(p2), "=&v"(p3) : "v"(voff), "s"(srd) : "memory");
  };
  auto gather_wait = [&](int voff) {
    int sp = 0;
    WAIT(true, 16) {
      asm volatile("s_waitcnt vmcnt(0)" : "+v"(p0), "+v"(p1), "+v"(p2), "+v"(p3) :: "memory");
      const unsigned want = tag;
      const u64 m0 = __builtin_amdgcn_ballot_w64(p0.y != want || p0.w != want), m1 = __builtin_amdgcn_ballot_w64(p1.y != want || p1.w != want);
      const u64 m2 = __builtin_amdgcn_ballot_w64(p2.y != want || p2.w != want), m3 = __builtin_amdgcn_ballot_w64(p3.y != want || p3.w != want);
      if (!(m0 | m1 | m2 | m3)) break;
      if (++sp > 16) __builtin_amdgcn_s_sleep(8);
      asm volatile("s_mov_b64 exec, %5\n\tbuffer_load_dwordx4 %0, %4, %9, 0 offen sc1\n\t"
                   "s_mov_b64 exec, %6\n\tbuffer_load_dwordx4 %1, %4, %9, 0 offen offset:1024 sc1\n\t"
                   "s_mov_b64 exec, %7\n\tbuffer_load_dwordx4 %2, %4, %9, 0 offen offset:2048 sc1\n\t"
                   "s_mov_b64 exec, %8\n\tbuffer_load_dwordx4 %3, %4, %9, 0 offen offset:3072 sc1\n\t"
                   "s_mov_b64 exec, -1"
                   : "+v"(p0), "+v"(p1), "+v"(p2), "+v"(p3) : "v"(voff), "s"(m0), "s"(m1), "s"(m2), "s"(m3), "s"(srd) : "memory");
    }
    asm volatile("s_waitcnt vmcnt(0)" : "+v"(p0), "+v"(p1), "+v"(p2), "+v"(p3) :: "memory");
  };
  {
    const int po = WS_PO + ((lane >> 3) * 16 + (rg >> 1)) * 4096 + ((rg & 1) * 128 + 32 * w) * 4 + (lane & 7) * 16;
    SPIN sp < 32 && (int)(now() - tgate) < 0; ++sp) __builtin_amdgcn_s_sleep(1);
    gather_issue(po);
    WAIT(RFL(ctl[11]) == 0u, 1) { __builtin_amdgcn_s_sleep(1); }
    asm volatile("" ::: "memory");
    v4u wa[4];
    {
      LAS unsigned char* wb = L + S_OFF + (8 * g + qq) * 128 + w * 32 + pp * 8;
#pragma unroll
      for (int k = 0; k < 4; ++k) { const v2u lo = trread(wb + k * 4096), hi = trread(wb + k * 4096 + 512); wa[k] = v4u{lo.x, lo.y, hi.x, hi.y}; }
    }
    const int yo = WS_Y2 + ((((g & 1) * 4 + (g >> 1)) * 7 + lc) * 4 + w) * 4096 + rg * 128 + r16 * 8;
    gather_wait(po);
    STAMP(9 + w);
    LAS unsigned char* xb = L + X_OFF + w * 2048 + lane * 8;
    *(LAS v2u*)(xb) = v2u{p0.x, p0.z}; *(LAS v2u*)(xb + 512) = v2u{p1.x, p1.z};
    *(LAS v2u*)(xb + 1024) = v2u{p2.x, p2.z}; *(LAS v2u*)(xb + 1536) = v2u{p3.x, p3.z};
    LDSW();
    __builtin_amdgcn_s_barrier();
    asm volatile("" ::: "memory");
    v4u oa[8];
#pragma unroll
    for (int k = 0; k < 8; ++k) oa[k] = *(const LAS v4u*)(L + X_OFF + k * 1024 + r16 * 64 + g * 16);
    f32x4 ya = {0.f, 0.f, 0.f, 0.f}, yb = {0.f, 0.f, 0.f, 0.f};
#pragma unroll
    for (int k = 0; k < 4; ++k) { ya = mfma(oa[2 * k], wa[k], ya); yb = mfma(oa[2 * k + 1], wa[k], yb); }
    ya += yb;
    auto t0 = __builtin_amdgcn_permlane32_swap(__float_as_uint(ya[0]), __float_as_uint(ya[1]), false, false);
    auto t1 = __builtin_amdgcn_permlane32_swap(__float_as_uint(ya[2]), __float_as_uint(ya[3]), false, false);
    const float v0 = __uint_as_float(t0[0]) + __uint_as_float(t0[1]), v1 = __uint_as_float(t1[0]) + __uint_as_float(t1[1]);
    __builtin_amdgcn_raw_buffer_store_b64(v2u{__float_as_uint(v0), tag}, rs, yo, 0, 16);
    __builtin_amdgcn_raw_buffer_store_b64(v2u{__float_as_uint(v1), tag}, rs, yo + 2 * 7 * 4 * 4096, 0, 16);
  }
  if (w >= S3W) { STAMP_FLUSH(); return; }
  STAMP(14);
  {
    const int nt = (bid >> 3) & 3;
    const int yo = WS_Y2 + ((b3 * 7 + lc) * 4 + nt) * 4096 + lane * 16;
    __builtin_amdgcn_s_sleep(D2);
    if (w & 1) __builtin_amdgcn_s_sleep(S3D);
    if (w & 2) __builtin_amdgcn_s_sleep(2 * S3D);
    gather_issue(yo);
    gather_wait(yo);
    STAMP(15);
    float v0 = (__uint_as_float(p0.x) + __uint_as_float(p1.x)) + (__uint_as_float(p2.x) + __uint_as_float(p3.x));
    float v1 = (__uint_as_float(p0.z) + __uint_as_float(p1.z)) + (__uint_as_float(p2.z) + __uint_as_float(p3.z));
    v0 += dpp<XOR8>(v0); v1 += dpp<XOR8>(v1);
    v0 = swap32add(swap16add(v0)); v1 = swap32add(swap16add(v1));
    const __amdgpu_buffer_rsrc_t ro = __builtin_amdgcn_make_buffer_rsrc((void*)out, 0, B * D * 2, 0x00020000);
    __builtin_amdgcn_raw_buffer_store_b32(cvtpk(v0, v1), ro, (lane < 8 && live) ? (b3 * D + lc * 64 + nt * 16 + 2 * lane) * 2 : B * D * 2, 0, OSC);
    __builtin_amdgcn_raw_buffer_store_b32(tag, rs, (bid == 0 && lane == 0 && live) ? WS_E : WS_BYTES, 0, 16);
    STAMP(13);
    STAMP_FLUSH();
  }
}

extern "C" void kernel_launch(void* const* d_in, const int* in_sizes, int n_in, void* d_out, int out_size, void* d_ws, size_t ws_size, hipStream_t stream) {
  (void)hipFuncSetAttribute((const void*)k_fused, hipFuncAttributeMaxDynamicSharedMemorySize, LDS_BYTES);
  k_fused<<<B * HKV * NSPLIT, NWAVE * 64, LDS_BYTES, stream>>>((const unsigned short*)d_in[0], (const unsigned short*)d_in[1], (const unsigned short*)d_in[2],
      (const unsigned short*)d_in[3], (const unsigned short*)d_in[4], (unsigned short*)d_out, (unsigned char*)d_ws);
}
```

```cpp
#include <hip/hip_runtime.h>
#include <hip/hip_bf16.h>
#include <stdint.h>

typedef unsigned v4u __attribute__((ext_vector_type(4)));
typedef unsigned v2u __attribute__((ext_vector_type(2)));
typedef int v4i __attribute__((ext_vector_type(4)));
typedef float f32x4 __attribute__((ext_vector_type(4)));
typedef float f32x2 __attribute__((ext_vector_type(2)));
typedef short s16x4 __attribute__((ext_vector_type(4)));
typedef __bf16 bf16x8 __attribute__((ext_vector_type(8)));
typedef __bf16 bf16x2 __attribute__((ext_vector_type(2)));
typedef unsigned long long u64;
#define LAS __attribute__((address_space(3)))
#define GAS __attribute__((address_space(1)))
#define RLX_AGENT __ATOMIC_RELAXED, __HIP_MEMORY_SCOPE_AGENT

constexpr int B = 8, D = 448, HKV = 8, SK = 8192, DH = 256;
constexpr int NSPLIT = 4;
constexpr int KPS = SK / NSPLIT;
#define TAU 876
#define NPRE 3
#define ST_AT 40
#define D1 0
#define D2 16
#define TH1 40
#define TH2 100
#define LEAD 10
#define PK0 8
#define PKG 12
#define PKD 3
#define QD 550
#define WO_AT 125
#define W0 24
#define ENDW 0
#define ENDV 1000
#define G1 165
#define G2 0
#define MAP2 1
#define XL 1
#define OT 0
#define OTA 116
#define OSC 0
#define N0 10
#define NPAIR 1
#define PCLIP 600
#define DCD 16
#define NSM 16
#define SMS (256 / NSM)
#define SMO (SMS / 2 - 1)
#define CAP0 215
#define CAP1 208
#define CAP2 192
#define SLACK 30
#define MIXA 4
#define MIXB 5
#define MIXO 0
#define S3W 1
#define S3D 4
#define WQ_AUX 2
constexpr int TILE_B = 16384, NSLOT = 8, NKT = KPS / 32, NT = 2 * NKT;
constexpr int NCONS = 4, NWAVE = 6;
constexpr int WAIT_BUDGET = 1 << 22;
constexpr int S_OFF = NSLOT * TILE_B;
constexpr int X_OFF = S_OFF + 2 * KPS * 4;
constexpr int P_OFF = X_OFF + 2048;
constexpr int Q_OFF = P_OFF + NKT * 128;
constexpr int C_OFF = Q_OFF + 1024;
constexpr int LDS_BYTES = C_OFF + 256;
constexpr int WS_E = 0;
constexpr int WS_QG = 4096;
constexpr int WS_ST = WS_QG + 8 * 2048 * 8;
constexpr int WS_PO = WS_ST + 8 * 16 * 4 * 16;
constexpr int WS_Y2 = WS_PO + 8 * 16 * 4 * 1024;
constexpr int WS_PH = WS_Y2 + 8 * 7 * 4 * 4096;
constexpr int WS_DC = WS_PH + 256 * 128;
constexpr int WS_PX = WS_DC + 256 * 128;
constexpr int WS_BYTES = WS_PX + 256 * 8192;

__device__ __forceinline__ unsigned cvtpk(float lo, float hi) { f32x2 v = {lo, hi}; return __builtin_bit_cast(unsigned, __builtin_convertvector(v, bf16x2)); }
__device__ __forceinline__ f32x4 mfma(v4u a, v4u b, f32x4 c) {
  return __builtin_amdgcn_mfma_f32_16x16x32_bf16(__builtin_bit_cast(bf16x8, a), __builtin_bit_cast(bf16x8, b), c, 0, 0, 0);
}
__device__ __forceinline__ v2u trread(LAS unsigned char* p) { return __builtin_bit_cast(v2u, __builtin_amdgcn_ds_read_tr16_b64_v4i16((LAS s16x4*)p)); }
__device__ __forceinline__ float x16max(float x) {
  auto s = __builtin_amdgcn_permlane16_swap(__float_as_uint(x), __float_as_uint(x), false, false);
  x = fmaxf(__uint_as_float(s[0]), __uint_as_float(s[1]));
  auto t = __builtin_amdgcn_permlane32_swap(__float_as_uint(x), __float_as_uint(x), false, false);
  return fmaxf(__uint_as_float(t[0]), __uint_as_float(t[1]));
}
template <int CTRL> __device__ __forceinline__ float dpp(float x) {
  return __builtin_bit_cast(float, __builtin_amdgcn_mov_dpp(__builtin_bit_cast(int, x), CTRL, 0xf, 0xf, true));
}
constexpr int XOR1 = 0xB1, XOR2 = 0x4E, XOR7 = 0x141, XOR8 = 0x128;
__device__ __forceinline__ float swap32add(float a) {
  auto t = __builtin_amdgcn_permlane32_swap(__float_as_uint(a), __float_as_uint(a), false, false);
  return __uint_as_float(t[0]) + __uint_as_float(t[1]);
}
__device__ __forceinline__ float swap16add(float a) {
  auto t = __builtin_amdgcn_permlane16_swap(__float_as_uint(a), __float_as_uint(a), false, false);
  return __uint_as_float(t[0]) + __uint_as_float(t[1]);
}
__device__ __forceinline__ float wavesum(float x) {
  x += dpp<XOR1>(x); x += dpp<XOR2>(x); x += dpp<XOR7>(x); x += dpp<XOR8>(x);
  return swap32add(swap16add(x));
}
__device__ __forceinline__ unsigned now() { return (unsigned)__builtin_amdgcn_s_memrealtime(); }
__device__ __forceinline__ int probe(u64 addr) {
  u64 t0, t1; unsigned v;
  asm volatile("s_memtime %0\n\ts_waitcnt lgkmcnt(0)\n\ts_load_dword %1, %3, 0x0\n\ts_waitcnt lgkmcnt(0)\n\ts_memtime %2\n\ts_waitcnt lgkmcnt(0)"
               : "=&s"(t0), "=&s"(v), "=&s"(t1) : "s"(addr) : "memory");
  return (int)(unsigned)(t1 - t0);
}
#define LDSW() asm volatile("s_waitcnt lgkmcnt(0)" ::: "memory")
#define DRAIN() asm volatile("s_waitcnt vmcnt(0)" ::: "memory")
#define PIN16(a) asm volatile("" : "+v"(a[0]), "+v"(a[1]), "+v"(a[2]), "+v"(a[3]), "+v"(a[4]), "+v"(a[5]), "+v"(a[6]), "+v"(a[7]), \
                        "+v"(a[8]), "+v"(a[9]), "+v"(a[10]), "+v"(a[11]), "+v"(a[12]), "+v"(a[13]), "+v"(a[14]), "+v"(a[15]) :: "memory")
#define RFL(x) __builtin_amdgcn_readfirstlane(x)
#define LD_SC1(off) __builtin_amdgcn_raw_buffer_load_b128(rs, (off), 0, 16)
#define ST_SC1(v, off) __builtin_amdgcn_raw_buffer_store_b128((v), rs, (off), 0, 16)
#define SPIN _Pragma("nounroll") for (int sp = 0;
#define WAIT(cond, cost) _Pragma("nounroll") for (; bud > 0 && (cond); bud -= (cost))

__global__ void __launch_bounds__(NWAVE * 64, 1) k_fused(const unsigned short* __restrict__ x, const unsigned short* __restrict__ kc, const unsigned short* __restrict__ vc,
                                              const unsigned short* __restrict__ wq, const unsigned short* __restrict__ wo, unsigned short* __restrict__ out, unsigned char* ws) {
  extern __shared__ __attribute__((aligned(16))) unsigned char lds[];
  LAS unsigned char* L = (LAS unsigned char*)lds;
  volatile LAS unsigned* ctl = (volatile LAS unsigned*)(L + C_OFF);
  const int bid = blockIdx.x;
  const int lane = threadIdx.x & 63, wave = RFL(threadIdx.x >> 6);
  int bud = WAIT_BUDGET;
#define STAMP(i)
#define STAMP_FLUSH()
  unsigned o0 = (lane >> 5) * 512 + (((lane & 31) ^ ((lane >> 5) << 1)) << 4);
  unsigned o1 = o0 ^ 64, o2 = o0 ^ 128, o3 = o0 ^ 192;
  const unsigned ldsbase = (unsigned)(size_t)L;
  const int pg = (bid >> 3) * 4 + (bid & 3), side = (bid >> 2) & 1;
  const u64 roff = ((u64)pg << 21) + (u64)side * (N0 * TILE_B);
  u64 src = (u64)kc + roff;
#define DMA4(S) "global_load_lds_dwordx4 %1, " S " nt\n\tglobal_load_lds_dwordx4 %2, " S " offset:1024 nt\n\t" \
                "global_load_lds_dwordx4 %3, " S " offset:2048 nt\n\tglobal_load_lds_dwordx4 %4, " S " offset:3072 nt\n\t"
  {
    unsigned keep, cnt, c0 = o0, c1 = o1, c2 = o2, c3 = o3;
    asm volatile("s_cmp_lg_u32 %8, 0\n\ts_cbranch_scc1 .Lskip%=\n\t"
                 "s_setprio 3\n\t"
                 "s_mov_b32 %0, m0\n\ts_mov_b32 m0, %7\n\ts_mov_b32 %5, 8\n"
                 ".Lloop%=:\n\t" DMA4("%6")
                 "s_add_u32 m0, m0, 0x1000\n\t"
                 "v_add_u32 %1, 0x1000, %1\n\tv_add_u32 %2, 0x1000, %2\n\tv_add_u32 %3, 0x1000, %3\n\tv_add_u32 %4, 0x1000, %4\n\t"
                 "s_sub_u32 %5, %5, 1\n\ts_cmp_lg_u32 %5, 0\n\ts_cbranch_scc1 .Lloop%=\n\t"
                 "s_mov_b32 m0, %0\n"
                 ".Lskip%=:"
                 : "=&s"(keep), "+v"(c0), "+v"(c1), "+v"(c2), "+v"(c3), "=&s"(cnt)
                 : "s"(src), "s"(ldsbase), "s"(wave) : "memory", "scc");
  }
  if (threadIdx.x < 64) ((LAS unsigned*)(L + C_OFF))[lane] = 0u;
  LDSW();
  __builtin_amdgcn_s_barrier();
  if (wave == 0) {
    src += 2 * TILE_B;
    unsigned gap = 8192;
    int pub = 0;
    unsigned phi = 0, tlast = now() * 4u - 4000u;
#pragma nounroll
    for (int t = 2; t < NT + 3; ++t) {
      if (t >= NSLOT && t < NT) {
        const unsigned needt = (unsigned)(t - NSLOT);
        WAIT(true, 1) {
          const v4u nd = *(volatile LAS v4u*)(L + C_OFF + 16);
          const unsigned mn = min(min(nd.x, nd.y), min(nd.z, nd.w));
          if (RFL(mn) > needt) break;
          __builtin_amdgcn_s_sleep(1);
        }
        asm volatile("" ::: "memory");
      }
      const v2u rf = *(volatile LAS v2u*)(L + C_OFF + 48);
      const unsigned rvalid = RFL(rf.x), ref = RFL(rf.y);
      const int e = t >= NT ? (t == NT ? TH2 + 1 : t == NT + 1 ? TH1 + 1 : 0) : t >= NT - ENDW ? ENDV : rvalid ? (int)(phi - ref) : 0;
      const unsigned tt = ((unsigned)t * TAU) >> 4;
      const int issued = t < NT ? t : NT;
      if (t == NKT) { src = (u64)vc + roff; gap = 8192; }
      if ((t & (NKT - 1)) == N0) {
        WAIT(RFL(ctl[34 + (t >> 6)]) == 0u, 1) { __builtin_amdgcn_s_sleep(1); }
        const unsigned c = (RFL(ctl[34 + (t >> 6)]) ^ (unsigned)side) & 1u;
        src = (u64)(t < NKT ? kc : vc) + ((u64)pg << 21) + 2 * N0 * TILE_B + c * 8192;
        gap = 16384;
      }
      int landed;
      if (e > TH2) { asm volatile("s_waitcnt vmcnt(47)" ::: "memory"); landed = issued - 3; }
      else if (t > NT + 1) { asm volatile("s_waitcnt vmcnt(16)" ::: "memory"); landed = issued - 1; }
      else if (e > TH1) { asm volatile("s_waitcnt vmcnt(32)" ::: "memory"); landed = issued - 2; }
      else { asm volatile("s_waitcnt vmcnt(%0)" :: "i"(W0) : "memory"); landed = issued - (W0 > 16 ? 2 : 1); }
      if (landed > pub) { pub = landed; ctl[0] = (unsigned)pub; }
      if (t < NT) {
        const unsigned cap = tlast + (e > TH2 ? CAP2 : e > TH1 ? CAP1 : CAP0);
        unsigned due = cap;
        const unsigned line = (ref + tt - LEAD) * 4u;
        if (rvalid && (int)(line - due) > 0) due = line;
        unsigned tn = now();
        SPIN sp < 48 && (int)(tn * 4u - due) < 0; ++sp) { __builtin_amdgcn_s_sleep(1); tn = now(); }
        tlast = (int)(tn * 4u - cap) < SLACK * 4 ? cap : tn * 4u - SLACK * 4;
        phi = tn - tt;
      }
      if (t < NT) {
        const unsigned dst = ldsbase + (t & (NSLOT - 1)) * TILE_B;
        const u64 s1 = src + 4096, s2 = src + gap, s3 = s2 + 4096;
        unsigned keep;
        asm volatile("s_mov_b32 %0, m0\n\t"
                     "s_mov_b32 m0, %9\n\ts_nop 0\n\t" DMA4("%5")
                     "s_add_u32 m0, %9, 0x1000\n\ts_nop 0\n\t" DMA4("%6")
                     "s_add_u32 m0, %9, 0x2000\n\ts_nop 0\n\t" DMA4("%7")
                     "s_add_u32 m0, %9, 0x3000\n\ts_nop 0\n\t" DMA4("%8")
                     "s_mov_b32 m0, %0"
                     : "=&s"(keep) : "v"(o0), "v"(o1), "v"(o2), "v"(o3), "s"(src), "s"(s1), "s"(s2), "s"(s3), "s"(dst) : "memory", "scc");
        src += 2 * gap;
      }
      ctl[1] = phi;
    }
    DRAIN();
    ctl[0] = (unsigned)NT;
    return;
  }
  const int s = (pg & 1) * 2 + side, kvh = (pg >> 1) & 7, b = pg >> 4;
  const bool has_out = (bid >> 5) < 7;
  const int lc = has_out ? (bid >> 5) : 0, rg = (bid & 7) * 4 + ((bid >> 3) & 3);
  const int b3 = bid & 7;
  auto wait_full = [&](unsigned n) {
    WAIT(RFL(ctl[0]) < n, 1) { __builtin_amdgcn_s_sleep(1); }
    asm volatile("" ::: "memory");
  };
  auto wait_full_slow = [&](unsigned n) {
    WAIT(RFL(ctl[0]) < n, 4) { __builtin_amdgcn_s_sleep(4); }
    asm volatile("" ::: "memory");
  };
  auto vote = [&]() {
    const unsigned hsh = now() * 0x9E3779B1u;
    const u64 pb = (u64)(side ? vc : kc) + ((u64)pg << 21) + (1 << 20) + (u64)(wave - 2) * 262144 + ((hsh >> 12) & 0x1FF80u & ~0x2000u);
    const unsigned f = (unsigned)(wave & 1) * 8192u;
    (void)probe(pb + 131072);
    int d = 0;
#pragma unroll
    for (int i = 0; i < NPAIR; ++i) { const u64 a = pb ^ (u64)(i * 0x5000); const int l0 = probe(a + f), l1 = probe(a + (f ^ 8192u)); d += min(max(l0 - l1, -PCLIP), PCLIP); }
    if ((wave ^ pg ^ side) & 1) d = -d;
    if (lane == 0) { __hip_atomic_fetch_add((LAS int*)(L + C_OFF) + 32, d, __ATOMIC_RELAXED, __HIP_MEMORY_SCOPE_WORKGROUP); LDSW(); __hip_atomic_fetch_add((LAS unsigned*)(L + C_OFF) + 33, 1u, __ATOMIC_RELAXED, __HIP_MEMORY_SCOPE_WORKGROUP); }
  };
  if (wave == 5) {
    unsigned eraw = __hip_atomic_load((GAS unsigned*)(ws + WS_E), RLX_AGENT);
    __builtin_amdgcn_sched_barrier(0);
    vote();
    asm volatile("" : "+v"(eraw));
    const unsigned tag = RFL(eraw) + 1u;
    *(volatile LAS v2u*)(L + C_OFF + 8) = v2u{1u, tag};
    {
      WAIT(RFL(ctl[33]) < 4u, 1) { __builtin_amdgcn_s_sleep(1); }
      if (lane == 0) __hip_atomic_store((GAS u64*)(ws + WS_DC) + bid * 16, ((u64)tag << 32) | ctl[32], RLX_AGENT);
      __builtin_amdgcn_s_sleep(DCD);
      u64 o = 0;
      WAIT(true, 16) {
        o = __hip_atomic_load((GAS u64*)(ws + WS_DC) + ((bid & ~7) + (lane & 7)) * 16, RLX_AGENT);
        if (__all((unsigned)(o >> 32) == tag)) break;
        __builtin_amdgcn_s_sleep(4);
      }
      float v = (float)(int)(unsigned)o;
      v += dpp<XOR1>(v); v += dpp<XOR2>(v);
      const unsigned ck = ((__builtin_amdgcn_readlane(__builtin_bit_cast(int, v), 0) > 0 ? 1u : 0u) ^ (unsigned)pg) & 1u;
      const unsigned cv = ((__builtin_amdgcn_readlane(__builtin_bit_cast(int, v), 4) > 0 ? 1u : 0u) ^ (unsigned)pg) & 1u;
      *(volatile LAS v2u*)(L + C_OFF + 136) = v2u{2u | ck, 2u | cv};
      const unsigned c = side ? cv : ck;
    }
    const bool sampled = (bid >> 3) % SMS == SMO;
#pragma nounroll
    for (int k = PK0; k + PKD < WO_AT; k += PKG) {
      wait_full_slow(k);
      const unsigned mine = RFL(ctl[1]);
      if (lane == 0 && sampled) __hip_atomic_store((GAS u64*)(ws + WS_PH) + bid * 16, ((u64)tag << 32) | mine, RLX_AGENT);
      wait_full_slow(k + PKD);
      u64 o = 0;
      if (lane < NSM) o = __hip_atomic_load((GAS u64*)(ws + WS_PH) + (8 * (SMS * (lane >> 3) + SMO) + (lane & 7)) * 16, RLX_AGENT);
      const bool ok = (unsigned)(o >> 32) == tag && lane < NSM;
      const float dsum = wavesum(ok ? (float)(int)((unsigned)o - mine) : 0.f);
      const int n = __builtin_popcountll(__builtin_amdgcn_ballot_w64(ok));
      if (n >= NSM / 2) { const unsigned ref = mine + (unsigned)(int)(dsum / (float)n); *(volatile LAS v2u*)(L + C_OFF + 48) = v2u{1u, ref}; }
    }
    if (!has_out) return;
    const unsigned char* src = (const unsigned char*)wo + (size_t)(rg * 128 + (lane >> 3)) * (D * 2) + lc * 128 + (lane & 7) * 16;
    wait_full_slow(WO_AT);
    WAIT(RFL(ctl[9]) < NCONS, 4) { __builtin_amdgcn_s_sleep(4); }
    asm volatile("" ::: "memory");
#pragma unroll
    for (int m = 0; m < 16; ++m) __builtin_amdgcn_global_load_lds((const GAS unsigned*)(src + m * 8 * (D * 2)), (LAS unsigned*)(L + S_OFF + m * 1024), 16, 0, 2);
    DRAIN();
    ctl[11] = 1u;
    return;
  }
  const __amdgpu_buffer_rsrc_t rs = __builtin_amdgcn_make_buffer_rsrc((void*)ws, 0, WS_BYTES, 0x00020000);
  const bool live = true;
  const int w = wave - 1;
  const int g = lane >> 4, r16 = lane & 15, head = lane & 1;
  const int qq = (lane >> 2) & 3, pp = lane & 3;
  unsigned tag = 0;
  f32x4 acc[4];
#pragma unroll
  for (int u = 0; u < 4; ++u) acc[u] = f32x4{0.f, 0.f, 0.f, 0.f};
  float oscale = 0.f;
  f32x4 acc0 = {0.f, 0.f, 0.f, 0.f};
  unsigned tgate = 0;
  {
    unsigned ka[4];
#pragma unroll
    for (int i = 0; i < 4; ++i) ka[i] = r16 * 512 + (((4 * i + g) ^ (2 * (r16 & 7))) << 4);
    v4u pre[NPRE][16];
    if (w == 0) {
      const unsigned tq0 = now();
      ctl[4] = (unsigned)(3 * NPRE);
      const int ctile = ((bid & 7) + 8 * (bid >> 5)) * 4 + ((bid >> 3) & 3);
      const unsigned short* wsrc = wq + (size_t)(lane >> 1) * 4096 + ctile * 16 + (lane & 1) * 8;
#pragma unroll
      for (int j = 0; j < 14; ++j) __builtin_amdgcn_global_load_lds((const GAS unsigned*)(wsrc + (size_t)j * 32 * 4096), (LAS unsigned*)(L + S_OFF + j * 1024), 16, 0, WQ_AUX);
      v4u xf[14];
#pragma unroll
      for (int j = 0; j < 7; ++j) __builtin_amdgcn_global_load_lds((const GAS unsigned*)(x + j * 512 + lane * 8), (LAS unsigned*)(L + X_OFF + j * 1024), 16, 0, 0);
      __builtin_amdgcn_sched_barrier(0);
      DRAIN();
#pragma unroll
      for (int j = 0; j < 14; ++j) xf[j] = *(const LAS v4u*)(L + X_OFF + (lane & 7) * (D * 2) + 64 * j + 16 * g);
      f32x4 d = {0.f, 0.f, 0.f, 0.f}, d2 = {0.f, 0.f, 0.f, 0.f};
      LAS unsigned char* wb = L + S_OFF + (8 * g + qq) * 32 + pp * 8;
      v4u wf[14];
#pragma unroll
      for (int j = 0; j < 14; ++j) { const v2u lo = trread(wb + j * 1024), hi = trread(wb + j * 1024 + 128); wf[j] = v4u{lo.x, lo.y, hi.x, hi.y}; }
      __builtin_amdgcn_sched_barrier(0);
#pragma unroll
      for (int j = 0; j < 14; j += 2) { d = mfma(wf[j], xf[j], d); d2 = mfma(wf[j + 1], xf[j + 1], d2); }
      d += d2;
      WAIT(RFL(ctl[2]) == 0u, 1) { __builtin_amdgcn_s_sleep(1); }
      tag = RFL(ctl[3]);
      if (r16 < 8) {
        const v4u gr = {cvtpk(d[0], d[1]), tag, cvtpk(d[2], d[3]), tag};
        ST_SC1(gr, WS_QG + (r16 * 2048 + ctile * 8 + 2 * g) * 8);
      }
      STAMP(3);
      const int qo = WS_QG + (b * 2048 + kvh * 256) * 8 + lane * 16;
      v4u ga = {0u, 0u, 0u, 0u}, gb = ga;
      SPIN sp < 1024 && (int)(now() - tq0) < QD; ++sp) __builtin_amdgcn_s_sleep(2);
      WAIT(true, 16) {
        ga = LD_SC1(qo);
        gb = LD_SC1(qo + 1024);
        const bool ok = ga.y == tag && ga.w == tag && gb.y == tag && gb.w == tag;
        if (__all(ok)) break;
        __builtin_amdgcn_s_sleep(8);
        asm volatile("" ::: "memory");
      }
      *(LAS v2u*)(L + Q_OFF + lane * 8) = v2u{ga.x, ga.z};
      *(LAS v2u*)(L + Q_OFF + 512 + lane * 8) = v2u{gb.x, gb.z};
      LDSW();
      ctl[10] = 1u;
      STAMP(4);
    } else {
      vote();
#pragma unroll
      for (int j = 0; j < NPRE; ++j) {
        const int t = (w - 1) + 3 * j;
        wait_full(t + 1);
        LAS unsigned char* tb = L + (t & (NSLOT - 1)) * TILE_B;
#pragma unroll
        for (int i = 0; i < 8; ++i) { pre[j][i] = *(const LAS v4u*)(tb + ka[i & 3] + (i >> 2) * 256); pre[j][8 + i] = *(const LAS v4u*)(tb + ka[i & 3] + (i >> 2) * 256 + 8192); }
        LDSW();
        ctl[4 + w] = (unsigned)(j + 1 < NPRE ? t + 3 : 3 * NPRE + w);
        PIN16(pre[j]);
      }
      WAIT(RFL(ctl[10]) == 0u, 2) { __builtin_amdgcn_s_sleep(2); }
      asm volatile("" ::: "memory");
      tag = RFL(ctl[3]);
    }
    v4u qf[8];
#pragma unroll
    for (int i = 0; i < 8; ++i) qf[i] = *(const LAS v4u*)(L + Q_OFF + head * 512 + 64 * i + 16 * g);
    float mx = -INFINITY;
    auto qk = [&](const v4u (&a)[16], int t) {
      f32x4 d0 = {0.f, 0.f, 0.f, 0.f}, d1 = {0.f, 0.f, 0.f, 0.f};
#pragma unroll
      for (int i = 0; i < 8; ++i) { d0 = mfma(a[i], qf[i], d0); d1 = mfma(a[8 + i], qf[i], d1); }
      mx = fmaxf(mx, fmaxf(fmaxf(fmaxf(d0[0], d0[1]), fmaxf(d0[2], d0[3])), fmaxf(fmaxf(d1[0], d1[1]), fmaxf(d1[2], d1[3]))));
      if (r16 < 4) {
        const f32x4 d = (r16 & 2) ? d1 : d0;
        *(LAS f32x4*)(L + S_OFF + head * (KPS * 4) + (t * 32 + (r16 & 2) * 8 + 4 * g) * 4) = d;
      }
    };
    if (w > 0) {
#pragma unroll
      for (int j = 0; j < NPRE; ++j) qk(pre[j], (w - 1) + 3 * j);
    }
#pragma nounroll
    for (int t = 3 * NPRE + w; t < NKT; t += NCONS) {
      wait_full(t + 1);
      LAS unsigned char* tb = L + (t & (NSLOT - 1)) * TILE_B;
      v4u a[16];
#pragma unroll
      for (int i = 0; i < 8; ++i) { a[i] = *(const LAS v4u*)(tb + ka[i & 3] + (i >> 2) * 256); a[8 + i] = *(const LAS v4u*)(tb + ka[i & 3] + (i >> 2) * 256 + 8192); }
      LDSW();
      ctl[4 + w] = (unsigned)(t + NCONS < NKT ? t + NCONS : NKT);
      PIN16(a);
      qk(a, t);
    }
    mx = x16max(mx);
    if (lane < 2) ((LAS float*)(L + C_OFF))[16 + w * 2 + lane] = mx;
    LDSW();
    if (lane == 0) __hip_atomic_fetch_add((LAS unsigned*)(L + C_OFF) + 8, 1u, __ATOMIC_RELAXED, __HIP_MEMORY_SCOPE_WORKGROUP);
    WAIT(RFL(ctl[8]) < NCONS, 1) { __builtin_amdgcn_s_sleep(1); }
    asm volatile("" ::: "memory");
    constexpr float C2 = 0.0625f * 1.4426950408889634f;
    bool swap;
    {
      const f32x4 ma = *(const LAS f32x4*)(L + C_OFF + 64), mb = *(const LAS f32x4*)(L + C_OFF + 80);
      const float M0 = fmaxf(fmaxf(ma[0], ma[2]), fmaxf(mb[0], mb[2])) * C2, M1 = fmaxf(fmaxf(ma[1], ma[3]), fmaxf(mb[1], mb[3])) * C2;
      const int tt = w * 64 + lane, tile = tt >> 2, gg = tt & 3;
      WAIT(RFL(ctl[34]) == 0u || RFL(ctl[35]) == 0u, 1) { __builtin_amdgcn_s_sleep(1); }
      swap = RFL(ctl[34]) != RFL(ctl[35]);
      float sum[2];
#pragma unroll
      for (int h = 0; h < 2; ++h) {
        const f32x4 sa = *(const LAS f32x4*)(L + S_OFF + h * (KPS * 4) + (tile * 32 + 4 * gg) * 4);
        const f32x4 sb = *(const LAS f32x4*)(L + S_OFF + h * (KPS * 4) + (tile * 32 + 16 + 4 * gg) * 4);
        const float mc = h ? M1 : M0;
        float p[8];
#pragma unroll
        for (int j = 0; j < 4; ++j) { p[j] = __builtin_amdgcn_exp2f(fmaf(sa[j], C2, -mc)); p[4 + j] = __builtin_amdgcn_exp2f(fmaf(sb[j], C2, -mc)); }
        sum[h] = ((p[0] + p[1]) + (p[2] + p[3])) + ((p[4] + p[5]) + (p[6] + p[7]));
        v4u pk = {cvtpk(p[0], p[1]), cvtpk(p[2], p[3]), cvtpk(p[4], p[5]), cvtpk(p[6], p[7])};
        *(LAS v4u*)(L + P_OFF + tile * 128 + gg * 32 + h * 16) = pk;
        if (swap) ST_SC1(pk, WS_PX + bid * 8192 + w * 2048 + h * 1024 + lane * 16);
      }
      sum[0] = wavesum(sum[0]); sum[1] = wavesum(sum[1]);
      if (lane < 2) ((LAS float*)(L + C_OFF))[24 + w * 2 + lane] = lane ? sum[1] : sum[0];
      DRAIN();
      LDSW();
      if (lane == 0) __hip_atomic_fetch_add((LAS unsigned*)(L + C_OFF) + 9, 1u, __ATOMIC_RELAXED, __HIP_MEMORY_SCOPE_WORKGROUP);
      WAIT(RFL(ctl[9]) < NCONS, 1) { __builtin_amdgcn_s_sleep(1); }
      asm volatile("" ::: "memory");
      if (w == 1 && lane < 2) {
        const f32x4 la = *(const LAS f32x4*)(L + C_OFF + 96), lb = *(const LAS f32x4*)(L + C_OFF + 112);
        const float Ls = lane ? ((la[1] + la[3]) + (lb[1] + lb[3])) : ((la[0] + la[2]) + (lb[0] + lb[2]));
        const v4u gr = {__float_as_uint(lane ? M1 : M0), tag, __float_as_uint(Ls), tag};
        ST_SC1(gr, WS_ST + ((b * 16 + kvh * 2 + lane) * 4 + s) * 16);
      }
    }
    const int key = 4 * g + qq;
    unsigned va[4];
#pragma unroll
    for (int u = 0; u < 4; ++u) va[u] = key * 512 + (((8 * w + 2 * u + (pp >> 1)) ^ (2 * (key & 7))) << 4) + (pp & 1) * 8;
    const unsigned pa = P_OFF + g * 32 + head * 16;
#pragma nounroll
    for (int t = 0; t < NKT; ++t) {
      if (t == N0) {
        v4u sc = {0u, 0u, 0u, 0u};
        WAIT(true, 16) {
          sc = LD_SC1(WS_ST + (b * 16 + kvh * 2) * 64 + (lane & 7) * 16);
          if (__all(sc.y == tag && sc.w == tag)) break;
          __builtin_amdgcn_s_sleep(8);
          asm volatile("" ::: "memory");
        }
        const float ms = __uint_as_float(sc.x), ls = __uint_as_float(sc.z);
        float M = fmaxf(ms, dpp<XOR1>(ms)); M = fmaxf(M, dpp<XOR2>(M));
        const float e = __builtin_amdgcn_exp2f(ms - M);
        float Lt = e * ls; Lt += dpp<XOR1>(Lt); Lt += dpp<XOR2>(Lt);
        const int wgt = __builtin_bit_cast(int, e / Lt);
        const int f0 = __builtin_amdgcn_readlane(wgt, s), f1 = __builtin_amdgcn_readlane(wgt, 4 + s);
        oscale = __builtin_bit_cast(float, head ? f1 : f0);
        if (swap) {
          asm volatile("" ::: "memory");
          const v4u x0 = LD_SC1(WS_PX + (bid ^ 4) * 8192 + w * 2048 + lane * 16), x1 = LD_SC1(WS_PX + (bid ^ 4) * 8192 + w * 2048 + 1024 + lane * 16);
          const int u = (r16 >> 1) & 3;
          const f32x4 a01 = (u & 1) ? acc[1] : acc[0], a23 = (u & 1) ? acc[3] : acc[2];
          acc0 = ((u & 2) ? a23 : a01) * oscale;
#pragma unroll
          for (int k = 0; k < 4; ++k) acc[k] = f32x4{0.f, 0.f, 0.f, 0.f};
          const int g0 = __builtin_amdgcn_readlane(wgt, s ^ 1), g1 = __builtin_amdgcn_readlane(wgt, 4 + (s ^ 1));
          oscale = __builtin_bit_cast(float, head ? g1 : g0);
          const int tt = w * 64 + lane;
          if (tt >= 4 * N0) { *(LAS v4u*)(L + P_OFF + tt * 32) = x0; *(LAS v4u*)(L + P_OFF + tt * 32 + 16) = x1; }
          LDSW();
          if (lane == 0) __hip_atomic_fetch_add((LAS unsigned*)(L + C_OFF) + 36, 1u, __ATOMIC_RELAXED, __HIP_MEMORY_SCOPE_WORKGROUP);
          WAIT(RFL(ctl[36]) < NCONS, 1) { __builtin_amdgcn_s_sleep(1); }
          asm volatile("" ::: "memory");
        }
      }
      v4u pf = *(const LAS v4u*)(L + pa + t * 128);
      if (t == NKT - 1) tgate = RFL(ctl[13]) + ((NT * TAU) >> 4) + G1;
      wait_full(NKT + t + 1);
      LAS unsigned char* tb = L + (t & (NSLOT - 1)) * TILE_B;
      v4u av[4];
#pragma unroll
      for (int u = 0; u < 4; ++u) { const v2u lo = trread(tb + va[u]), hi = trread(tb + va[u] + 8192); av[u] = v4u{lo.x, lo.y, hi.x, hi.y}; }
      LDSW();
      ctl[4 + w] = (unsigned)(NKT + t + 1);
      asm volatile("" : "+v"(av[0]), "+v"(av[1]), "+v"(av[2]), "+v"(av[3]), "+v"(pf) :: "memory");
#pragma unroll
      for (int u = 0; u < 4; ++u) acc[u] = mfma(av[u], pf, acc[u]);
    }
  }
  STAMP(5 + w);
  {
    const int u = (r16 >> 1) & 3;
    const f32x4 a01 = (u & 1) ? acc[1] : acc[0], a23 = (u & 1) ? acc[3] : acc[2];
    const f32x4 a = ((u & 2) ? a23 : a01) * oscale + acc0;
    const v4u o = {cvtpk(a[0], a[1]), tag, cvtpk(a[2], a[3]), tag};
    const int off = WS_PO + ((b * 16 + kvh * 2 + head) * 4 + s) * 1024 + (64 * w + 16 * u + 4 * g) * 4;
    ST_SC1(o, (r16 < 8 && live) ? off : WS_BYTES);
  }
  if (!has_out) { STAMP_FLUSH(); return; }
  const u64 wsa = (u64)ws;
  const v4i srd = {(int)(unsigned)wsa, (int)((unsigned)(wsa >> 32) & 0xffffu), WS_BYTES, 0x00020000};
  v4u p0, p1, p2, p3;
  auto gather_issue = [&](int voff) {
    asm volatile("buffer_load_dwordx4 %0, %4, %5, 0 offen sc1\n\t"
                 "buffer_load_dwordx4 %1, %4, %5, 0 offen offset:1024 sc1\n\t"
                 "buffer_load_dwordx4 %2, %4, %5, 0 offen offset:2048 sc1\n\t"
                 "buffer_load_dwordx4 %3, %4, %5, 0 offen offset:3072 sc1"
                 : "=&v"(p0), "=&v"(p1), "=&v"(p2), "=&v"(p3) : "v"(voff), "s"(srd) : "memory");
  };
  auto gather_wait = [&](int voff) {
    int sp = 0;
    WAIT(true, 16) {
      asm volatile("s_waitcnt vmcnt(0)" : "+v"(p0), "+v"(p1), "+v"(p2), "+v"(p3) :: "memory");
      const unsigned want = tag;
      const u64 m0 = __builtin_amdgcn_ballot_w64(p0.y != want || p0.w != want), m1 = __builtin_amdgcn_ballot_w64(p1.y != want || p1.w != want);
      const u64 m2 = __builtin_amdgcn_ballot_w64(p2.y != want || p2.w != want), m3 = __builtin_amdgcn_ballot_w64(p3.y != want || p3.w != want);
      if (!(m0 | m1 | m2 | m3)) break;
      if (++sp > 16) __builtin_amdgcn_s_sleep(8);
      asm volatile("s_mov_b64 exec, %5\n\tbuffer_load_dwordx4 %0, %4, %9, 0 offen sc1\n\t"
                   "s_mov_b64 exec, %6\n\tbuffer_load_dwordx4 %1, %4, %9, 0 offen offset:1024 sc1\n\t"
                   "s_mov_b64 exec, %7\n\tbuffer_load_dwordx4 %2, %4, %9, 0 offen offset:2048 sc1\n\t"
                   "s_mov_b64 exec, %8\n\tbuffer_load_dwordx4 %3, %4, %9, 0 offen offset:3072 sc1\n\t"
                   "s_mov_b64 exec, -1"
                   : "+v"(p0), "+v"(p1), "+v"(p2), "+v"(p3) : "v"(voff), "s"(m0), "s"(m1), "s"(m2), "s"(m3), "s"(srd) : "memory");
    }
    asm volatile("s_waitcnt vmcnt(0)" : "+v"(p0), "+v"(p1), "+v"(p2), "+v"(p3) :: "memory");
  };
  {
    const int po = WS_PO + ((lane >> 3) * 16 + (rg >> 1)) * 4096 + ((rg & 1) * 128 + 32 * w) * 4 + (lane & 7) * 16;
    SPIN sp < 32 && (int)(now() - tgate) < 0; ++sp) __builtin_amdgcn_s_sleep(1);
    gather_issue(po);
    WAIT(RFL(ctl[11]) == 0u, 1) { __builtin_amdgcn_s_sleep(1); }
    asm volatile("" ::: "memory");
    v4u wa[4];
    {
      LAS unsigned char* wb = L + S_OFF + (8 * g + qq) * 128 + w * 32 + pp * 8;
#pragma unroll
      for (int k = 0; k < 4; ++k) { const v2u lo = trread(wb + k * 4096), hi = trread(wb + k * 4096 + 512); wa[k] = v4u{lo.x, lo.y, hi.x, hi.y}; }
    }
    const int yo = WS_Y2 + ((((g & 1) * 4 + (g >> 1)) * 7 + lc) * 4 + w) * 4096 + rg * 128 + r16 * 8;
    gather_wait(po);
    STAMP(9 + w);
    LAS unsigned char* xb = L + X_OFF + w * 2048 + lane * 8;
    *(LAS v2u*)(xb) = v2u{p0.x, p0.z}; *(LAS v2u*)(xb + 512) = v2u{p1.x, p1.z};
    *(LAS v2u*)(xb + 1024) = v2u{p2.x, p2.z}; *(LAS v2u*)(xb + 1536) = v2u{p3.x, p3.z};
    LDSW();
    __builtin_amdgcn_s_barrier();
    asm volatile("" ::: "memory");
    v4u oa[8];
#pragma unroll
    for (int k = 0; k < 8; ++k) oa[k] = *(const LAS v4u*)(L + X_OFF + k * 1024 + r16 * 64 + g * 16);
    f32x4 ya = {0.f, 0.f, 0.f, 0.f}, yb = {0.f, 0.f, 0.f, 0.f};
#pragma unroll
    for (int k = 0; k < 4; ++k) { ya = mfma(oa[2 * k], wa[k], ya); yb = mfma(oa[2 * k + 1], wa[k], yb); }
    ya += yb;
    auto t0 = __builtin_amdgcn_permlane32_swap(__float_as_uint(ya[0]), __float_as_uint(ya[1]), false, false);
    auto t1 = __builtin_amdgcn_permlane32_swap(__float_as_uint(ya[2]), __float_as_uint(ya[3]), false, false);
    const float v0 = __uint_as_float(t0[0]) + __uint_as_float(t0[1]), v1 = __uint_as_float(t1[0]) + __uint_as_float(t1[1]);
    __builtin_amdgcn_raw_buffer_store_b64(v2u{__float_as_uint(v0), tag}, rs, yo, 0, 16);
    __builtin_amdgcn_raw_buffer_store_b64(v2u{__float_as_uint(v1), tag}, rs, yo + 2 * 7 * 4 * 4096, 0, 16);
  }
  if (w >= S3W) { STAMP_FLUSH(); return; }
  STAMP(14);
  {
    const int nt = (bid >> 3) & 3;
    const int yo = WS_Y2 + ((b3 * 7 + lc) * 4 + nt) * 4096 + lane * 16;
    __builtin_amdgcn_s_sleep(D2);
    if (w & 1) __builtin_amdgcn_s_sleep(S3D);
    if (w & 2) __builtin_amdgcn_s_sleep(2 * S3D);
    gather_issue(yo);
    gather_wait(yo);
    STAMP(15);
    float v0 = (__uint_as_float(p0.x) + __uint_as_float(p1.x)) + (__uint_as_float(p2.x) + __uint_as_float(p3.x));
    float v1 = (__uint_as_float(p0.z) + __uint_as_float(p1.z)) + (__uint_as_float(p2.z) + __uint_as_float(p3.z));
    v0 += dpp<XOR8>(v0); v1 += dpp<XOR8>(v1);
    v0 = swap32add(swap16add(v0)); v1 = swap32add(swap16add(v1));
    const __amdgpu_buffer_rsrc_t ro = __builtin_amdgcn_make_buffer_rsrc((void*)out, 0, B * D * 2, 0x00020000);
    __builtin_amdgcn_raw_buffer_store_b32(cvtpk(v0, v1), ro, (lane < 8 && live) ? (b3 * D + lc * 64 + nt * 16 + 2 * lane) * 2 : B * D * 2, 0, OSC);
    __builtin_amdgcn_raw_buffer_store_b32(tag, rs, (bid == 0 && lane == 0 && live) ? WS_E : WS_BYTES, 0, 16);
    STAMP(13);
    STAMP_FLUSH();
  }
}

extern "C" void kernel_launch(void* const* d_in, const int* in_sizes, int n_in, void* d_out, int out_size, void* d_ws, size_t ws_size, hipStream_t stream) {
  (void)hipFuncSetAttribute((const void*)k_fused, hipFuncAttributeMaxDynamicSharedMemorySize, LDS_BYTES);
  k_fused<<<B * HKV * NSPLIT, NWAVE * 64, LDS_BYTES, stream>>>((const unsigned short*)d_in[0], (const unsigned short*)d_in[1], (const unsigned short*)d_in[2],
      (const unsigned short*)d_in[3], (const unsigned short*)d_in[4], (unsigned short*)d_out, (unsigned char*)d_ws);
}
```

```cpp
#include <hip/hip_runtime.h>
#include <hip/hip_bf16.h>
#include <stdint.h>

typedef unsigned v4u __attribute__((ext_vector_type(4)));
typedef unsigned v2u __attribute__((ext_vector_type(2)));
typedef int v4i __attribute__((ext_vector_type(4)));
typedef float f32x4 __attribute__((ext_vector_type(4)));
typedef float f32x2 __attribute__((ext_vector_type(2)));
typedef short s16x4 __attribute__((ext_vector_type(4)));
typedef __bf16 bf16x8 __attribute__((ext_vector_type(8)));
typedef __bf16 bf16x2 __attribute__((ext_vector_type(2)));
typedef unsigned long long u64;
#define LAS __attribute__((address_space(3)))
#define GAS __attribute__((address_space(1)))
#define RLX_AGENT __ATOMIC_RELAXED, __HIP_MEMORY_SCOPE_AGENT

constexpr int B = 8, D = 448, HKV = 8, SK = 8192, DH = 256;
constexpr int NSPLIT = 4;
constexpr int KPS = SK / NSPLIT;
#define TAU 876
#define NPRE 3
#define ST_AT 40
#define D1 0
#define D2 16
#define TH1 25
#define TH2 120
#define LEAD 30
#define PK0 8
#define PKG 24
#define PKD 3
#define QD 550
#define WO_AT 126
#define W0 16
#define ENDW 0
#define ENDV 1000
#define G1 150
#define G2 0
#define MAP2 1
#define XL 1
#define OT 0
#define OTA 116
#define OSC 0
#define XMF (bid & 1)
#define MIXA 4
#define MIXB 5
#define MIXO 0
#define S3W 1
#define S3D 4
#define WQ_AUX 2
constexpr int TILE_B = 16384, NSLOT = 8, NKT = KPS / 32, NT = 2 * NKT;
constexpr int NCONS = 4, NWAVE = 6;
constexpr int WAIT_BUDGET = 1 << 22;
constexpr int S_OFF = NSLOT * TILE_B;
constexpr int X_OFF = S_OFF + 2 * KPS * 4;
constexpr int P_OFF = X_OFF + 2048;
constexpr int Q_OFF = P_OFF + NKT * 128;
constexpr int C_OFF = Q_OFF + 1024;
constexpr int LDS_BYTES = C_OFF + 256;
constexpr int WS_E = 0;
constexpr int WS_QG = 4096;
constexpr int WS_ST = WS_QG + 8 * 2048 * 8;
constexpr int WS_PO = WS_ST + 8 * 16 * 4 * 16;
constexpr int WS_Y2 = WS_PO + 8 * 16 * 4 * 1024;
constexpr int WS_PH = WS_Y2 + 8 * 7 * 4 * 4096;
constexpr int WS_BYTES = WS_PH + 256 * 128;

__device__ __forceinline__ unsigned cvtpk(float lo, float hi) { f32x2 v = {lo, hi}; return __builtin_bit_cast(unsigned, __builtin_convertvector(v, bf16x2)); }
__device__ __forceinline__ f32x4 mfma(v4u a, v4u b, f32x4 c) {
  return __builtin_amdgcn_mfma_f32_16x16x32_bf16(__builtin_bit_cast(bf16x8, a), __builtin_bit_cast(bf16x8, b), c, 0, 0, 0);
}
__device__ __forceinline__ v2u trread(LAS unsigned char* p) { return __builtin_bit_cast(v2u, __builtin_amdgcn_ds_read_tr16_b64_v4i16((LAS s16x4*)p)); }
__device__ __forceinline__ float x16max(float x) {
  auto s = __builtin_amdgcn_permlane16_swap(__float_as_uint(x), __float_as_uint(x), false, false);
  x = fmaxf(__uint_as_float(s[0]), __uint_as_float(s[1]));
  auto t = __builtin_amdgcn_permlane32_swap(__float_as_uint(x), __float_as_uint(x), false, false);
  return fmaxf(__uint_as_float(t[0]), __uint_as_float(t[1]));
}
template <int CTRL> __device__ __forceinline__ float dpp(float x) {
  return __builtin_bit_cast(float, __builtin_amdgcn_mov_dpp(__builtin_bit_cast(int, x), CTRL, 0xf, 0xf, true));
}
constexpr int XOR1 = 0xB1, XOR2 = 0x4E, XOR7 = 0x141, XOR8 = 0x128;
__device__ __forceinline__ float swap32add(float a) {
  auto t = __builtin_amdgcn_permlane32_swap(__float_as_uint(a), __float_as_uint(a), false, false);
  return __uint_as_float(t[0]) + __uint_as_float(t[1]);
}
__device__ __forceinline__ float swap16add(float a) {
  auto t = __builtin_amdgcn_permlane16_swap(__float_as_uint(a), __float_as_uint(a), false, false);
  return __uint_as_float(t[0]) + __uint_as_float(t[1]);
}
__device__ __forceinline__ float wavesum(float x) {
  x += dpp<XOR1>(x); x += dpp<XOR2>(x); x += dpp<XOR7>(x); x += dpp<XOR8>(x);
  return swap32add(swap16add(x));
}
__device__ __forceinline__ unsigned now() { return (unsigned)__builtin_amdgcn_s_memrealtime(); }
#define LDSW() asm volatile("s_waitcnt lgkmcnt(0)" ::: "memory")
#define DRAIN() asm volatile("s_waitcnt vmcnt(0)" ::: "memory")
#define PIN16(a) asm volatile("" : "+v"(a[0]), "+v"(a[1]), "+v"(a[2]), "+v"(a[3]), "+v"(a[4]), "+v"(a[5]), "+v"(a[6]), "+v"(a[7]), \
                        "+v"(a[8]), "+v"(a[9]), "+v"(a[10]), "+v"(a[11]), "+v"(a[12]), "+v"(a[13]), "+v"(a[14]), "+v"(a[15]) :: "memory")
#define RFL(x) __builtin_amdgcn_readfirstlane(x)
#define LD_SC1(off) __builtin_amdgcn_raw_buffer_load_b128(rs, (off), 0, 16)
#define ST_SC1(v, off) __builtin_amdgcn_raw_buffer_store_b128((v), rs, (off), 0, 16)
#define SPIN _Pragma("nounroll") for (int sp = 0;
#define WAIT(cond, cost) _Pragma("nounroll") for (; bud > 0 && (cond); bud -= (cost))

__global__ void __launch_bounds__(NWAVE * 64, 1) k_fused(const unsigned short* __restrict__ x, const unsigned short* __restrict__ kc, const unsigned short* __restrict__ vc,
                                              const unsigned short* __restrict__ wq, const unsigned short* __restrict__ wo, unsigned short* __restrict__ out, unsigned char* ws) {
  extern __shared__ __attribute__((aligned(16))) unsigned char lds[];
  LAS unsigned char* L = (LAS unsigned char*)lds;
  volatile LAS unsigned* ctl = (volatile LAS unsigned*)(L + C_OFF);
  const int bid = blockIdx.x;
  const int lane = threadIdx.x & 63, wave = RFL(threadIdx.x >> 6);
  int bud = WAIT_BUDGET;
#define STAMP(i)
#define STAMP_FLUSH()
  unsigned o0 = (lane >> 5) * 512 + (((lane & 31) ^ ((lane >> 5) << 1)) << 4);
  unsigned o1 = o0 ^ 64, o2 = o0 ^ 128, o3 = o0 ^ 192;
  const unsigned ldsbase = (unsigned)(size_t)L;
  const unsigned xm = (unsigned)(XMF) * TILE_B;
  u64 src = (u64)kc + (u64)bid * (KPS * DH * 2);
#define DMA4(S) "global_load_lds_dwordx4 %1, " S " nt\n\tglobal_load_lds_dwordx4 %2, " S " offset:1024 nt\n\t" \
                "global_load_lds_dwordx4 %3, " S " offset:2048 nt\n\tglobal_load_lds_dwordx4 %4, " S " offset:3072 nt\n\t"
  {
    unsigned keep, cnt, c0 = o0, c1 = o1, c2 = o2, c3 = o3;
    asm volatile("s_cmp_lg_u32 %8, 0\n\ts_cbranch_scc1 .Lskip%=\n\t"
                 "s_setprio 3\n\t"
                 "s_mov_b32 %0, m0\n\ts_mov_b32 m0, %7\n\ts_mov_b32 %5, 8\n"
                 ".Lloop%=:\n\t" DMA4("%6")
                 "s_add_u32 m0, m0, 0x1000\n\t"
                 "v_add_u32 %1, 0x1000, %1\n\tv_add_u32 %2, 0x1000, %2\n\tv_add_u32 %3, 0x1000, %3\n\tv_add_u32 %4, 0x1000, %4\n\t"
                 "s_sub_u32 %5, %5, 1\n\ts_cmp_lg_u32 %5, 0\n\ts_cbranch_scc1 .Lloop%=\n\t"
                 "s_mov_b32 m0, %0\n"
                 ".Lskip%=:"
                 : "=&s"(keep), "+v"(c0), "+v"(c1), "+v"(c2), "+v"(c3), "=&s"(cnt)
                 : "s"(src + (xm & ~(unsigned)TILE_B)), "s"(ldsbase), "s"(wave) : "memory", "scc");
  }
  if (threadIdx.x < 64) ((LAS unsigned*)(L + C_OFF))[lane] = 0u;
  LDSW();
  __builtin_amdgcn_s_barrier();
  if (wave == 0) {
    const u64 vb = (u64)vc + (u64)bid * (KPS * DH * 2);
    int pub = 0;
    unsigned phi = 0;
#pragma nounroll
    for (int t = 2; t < NT + 3; ++t) {
      if (t >= NSLOT && t < NT) {
        const unsigned needt = (unsigned)(t - NSLOT);
        WAIT(true, 1) {
          const v4u nd = *(volatile LAS v4u*)(L + C_OFF + 16);
          const unsigned mn = min(min(nd.x, nd.y), min(nd.z, nd.w));
          if (RFL(mn) > needt) break;
          __builtin_amdgcn_s_sleep(1);
        }
        asm volatile("" ::: "memory");
      }
      const v2u rf = *(volatile LAS v2u*)(L + C_OFF + 48);
      const unsigned rvalid = RFL(rf.x), ref = RFL(rf.y);
      const int e = t >= NT ? (t == NT ? TH2 + 1 : t == NT + 1 ? TH1 + 1 : 0) : t >= NT - ENDW ? ENDV : rvalid ? (int)(phi - ref) : 0;
      const unsigned tt = ((unsigned)t * TAU) >> 4;
      const int issued = t < NT ? t : NT;
      if (t == NKT) src = vb;
      int landed; unsigned adj;
      if (e > TH2) { asm volatile("s_waitcnt vmcnt(47)" ::: "memory"); landed = issued - 3; adj = ((47 - W0) * TAU) >> 8; }
      else if (e > TH1) { asm volatile("s_waitcnt vmcnt(32)" ::: "memory"); landed = issued - 2; adj = ((32 - W0) * TAU) >> 8; }
      else if (W0 > 16 && t > NT + 1) { asm volatile("s_waitcnt vmcnt(16)" ::: "memory"); landed = issued - 1; adj = 0; }
      else {
        asm volatile("s_waitcnt vmcnt(%0)" :: "i"(W0) : "memory"); landed = issued - (W0 > 16 ? 2 : 1); adj = 0;
        if (e < 0) { const unsigned due = ref + tt - LEAD; SPIN sp < 64 && (int)(now() - due) < 0; ++sp) __builtin_amdgcn_s_sleep(1); }
      }
      if (t < NT) {
        const unsigned dst = ldsbase + (t & (NSLOT - 1)) * TILE_B;
        const unsigned ti = (unsigned)(t & (NKT - 1));
        const u64 s0 = src + ((ti * TILE_B) ^ (ti < 2 ? xm & ~(unsigned)TILE_B : xm));
        const u64 s1 = s0 + 4096, s2 = s0 + 8192, s3 = s0 + 12288;
        unsigned keep;
        asm volatile("s_mov_b32 %0, m0\n\t"
                     "s_mov_b32 m0, %9\n\ts_nop 0\n\t" DMA4("%5")
                     "s_add_u32 m0, %9, 0x1000\n\ts_nop 0\n\t" DMA4("%6")
                     "s_add_u32 m0, %9, 0x2000\n\ts_nop 0\n\t" DMA4("%7")
                     "s_add_u32 m0, %9, 0x3000\n\ts_nop 0\n\t" DMA4("%8")
                     "s_mov_b32 m0, %0"
                     : "=&s"(keep) : "v"(o0), "v"(o1), "v"(o2), "v"(o3), "s"(s0), "s"(s1), "s"(s2), "s"(s3), "s"(dst) : "memory", "scc");
      }
      if (landed > pub) pub = landed;
      phi = now() - tt + adj;
      *(volatile LAS v2u*)(L + C_OFF) = v2u{(unsigned)pub, phi};
    }
    DRAIN();
    ctl[0] = (unsigned)NT;
    return;
  }
  const int s = bid & 3, kvh = (bid >> 2) & 7, b = bid >> 5;
  const bool has_out = (bid >> 5) < 7;
  const int lc = has_out ? (bid >> 5) : 0, rg = (bid & 7) * 4 + ((bid >> 3) & 3);
  const int b3 = bid & 7;
  auto wait_full = [&](unsigned n) {
    WAIT(RFL(ctl[0]) < n, 1) { __builtin_amdgcn_s_sleep(1); }
    asm volatile("" ::: "memory");
  };
  auto wait_full_slow = [&](unsigned n) {
    WAIT(RFL(ctl[0]) < n, 4) { __builtin_amdgcn_s_sleep(4); }
    asm volatile("" ::: "memory");
  };
  if (wave == 5) {
    WAIT(RFL(ctl[2]) == 0u, 4) { __builtin_amdgcn_s_sleep(4); }
    const unsigned tag = RFL(ctl[3]);
    int mixs = MIXA;
#pragma nounroll
    for (int k = PK0; k + PKD < WO_AT; k += PKG) {
      wait_full_slow(k);
      const unsigned mine = RFL(ctl[1]);
      if (lane == 0) __hip_atomic_store((GAS u64*)(ws + WS_PH) + bid * 16, ((u64)tag << 32) | mine, RLX_AGENT);
      wait_full_slow(k + PKD);
      u64 o = 0;
      if (lane < 8) o = __hip_atomic_load((GAS u64*)(ws + WS_PH) + (((((bid >> 3) + mixs * (lane + MIXO)) & 31) << 3) + lane) * 16, RLX_AGENT);
      mixs = MIXB - mixs;
      const bool ok = (unsigned)(o >> 32) == tag && lane < 8;
      const float dsum = wavesum(ok ? (float)(int)((unsigned)o - mine) : 0.f);
      const int n = __builtin_popcountll(__builtin_amdgcn_ballot_w64(ok));
      if (n >= 4) { const unsigned ref = mine + (unsigned)(int)(dsum / (float)n); *(volatile LAS v2u*)(L + C_OFF + 48) = v2u{1u, ref}; }
    }
    if (!has_out) return;
    const unsigned char* src = (const unsigned char*)wo + (size_t)(rg * 128 + (lane >> 3)) * (D * 2) + lc * 128 + (lane & 7) * 16;
    wait_full_slow(WO_AT);
    WAIT(RFL(ctl[9]) < NCONS, 4) { __builtin_amdgcn_s_sleep(4); }
    asm volatile("" ::: "memory");
#pragma unroll
    for (int m = 0; m < 16; ++m) __builtin_amdgcn_global_load_lds((const GAS unsigned*)(src + m * 8 * (D * 2)), (LAS unsigned*)(L + S_OFF + m * 1024), 16, 0, 2);
    DRAIN();
    ctl[11] = 1u;
    return;
  }
  const __amdgpu_buffer_rsrc_t rs = __builtin_amdgcn_make_buffer_rsrc((void*)ws, 0, WS_BYTES, 0x00020000);
  const bool live = true;
  const int w = wave - 1;
  const int g = lane >> 4, r16 = lane & 15, head = lane & 1;
  const int qq = (lane >> 2) & 3, pp = lane & 3;
  unsigned tag = 0;
  f32x4 acc[4];
#pragma unroll
  for (int u = 0; u < 4; ++u) acc[u] = f32x4{0.f, 0.f, 0.f, 0.f};
  float oscale = 0.f;
  unsigned tgate = 0;
  {
    unsigned ka[4];
#pragma unroll
    for (int i = 0; i < 4; ++i) ka[i] = r16 * 512 + (((4 * i + g) ^ (2 * (r16 & 7))) << 4);
    v4u pre[NPRE][16];
    if (w == 0) {
      const unsigned tq0 = now();
      ctl[4] = (unsigned)(3 * NPRE);
      unsigned eraw = __hip_atomic_load((GAS unsigned*)(ws + WS_E), RLX_AGENT);
      const int ctile = ((bid & 7) + 8 * (bid >> 5)) * 4 + ((bid >> 3) & 3);
      const unsigned short* wsrc = wq + (size_t)(lane >> 1) * 4096 + ctile * 16 + (lane & 1) * 8;
#pragma unroll
      for (int j = 0; j < 14; ++j) __builtin_amdgcn_global_load_lds((const GAS unsigned*)(wsrc + (size_t)j * 32 * 4096), (LAS unsigned*)(L + S_OFF + j * 1024), 16, 0, WQ_AUX);
      v4u xf[14];
#pragma unroll
      for (int j = 0; j < 7; ++j) __builtin_amdgcn_global_load_lds((const GAS unsigned*)(x + j * 512 + lane * 8), (LAS unsigned*)(L + X_OFF + j * 1024), 16, 0, 0);
      __builtin_amdgcn_sched_barrier(0);
      DRAIN();
#pragma unroll
      for (int j = 0; j < 14; ++j) xf[j] = *(const LAS v4u*)(L + X_OFF + (lane & 7) * (D * 2) + 64 * j + 16 * g);
      asm volatile("" : "+v"(eraw));
      tag = RFL(eraw) + 1u;
      *(volatile LAS v2u*)(L + C_OFF + 8) = v2u{1u, tag};
      f32x4 d = {0.f, 0.f, 0.f, 0.f}, d2 = {0.f, 0.f, 0.f, 0.f};
      LAS unsigned char* wb = L + S_OFF + (8 * g + qq) * 32 + pp * 8;
      v4u wf[14];
#pragma unroll
      for (int j = 0; j < 14; ++j) { const v2u lo = trread(wb + j * 1024), hi = trread(wb + j * 1024 + 128); wf[j] = v4u{lo.x, lo.y, hi.x, hi.y}; }
      __builtin_amdgcn_sched_barrier(0);
#pragma unroll
      for (int j = 0; j < 14; j += 2) { d = mfma(wf[j], xf[j], d); d2 = mfma(wf[j + 1], xf[j + 1], d2); }
      d += d2;
      if (r16 < 8) {
        const v4u gr = {cvtpk(d[0], d[1]), tag, cvtpk(d[2], d[3]), tag};
        ST_SC1(gr, WS_QG + (r16 * 2048 + ctile * 8 + 2 * g) * 8);
      }
      STAMP(3);
      const int qo = WS_QG + (b * 2048 + kvh * 256) * 8 + lane * 16;
      v4u ga = {0u, 0u, 0u, 0u}, gb = ga;
      SPIN sp < 1024 && (int)(now() - tq0) < QD; ++sp) __builtin_amdgcn_s_sleep(2);
      WAIT(true, 16) {
        ga = LD_SC1(qo);
        gb = LD_SC1(qo + 1024);
        const bool ok = ga.y == tag && ga.w == tag && gb.y == tag && gb.w == tag;
        if (__all(ok)) break;
        __builtin_amdgcn_s_sleep(8);
        asm volatile("" ::: "memory");
      }
      *(LAS v2u*)(L + Q_OFF + lane * 8) = v2u{ga.x, ga.z};
      *(LAS v2u*)(L + Q_OFF + 512 + lane * 8) = v2u{gb.x, gb.z};
      LDSW();
      ctl[10] = 1u;
      STAMP(4);
    } else {
#pragma unroll
      for (int j = 0; j < NPRE; ++j) {
        const int t = (w - 1) + 3 * j;
        wait_full(t + 1);
        LAS unsigned char* tb = L + (t & (NSLOT - 1)) * TILE_B;
#pragma unroll
        for (int i = 0; i < 8; ++i) { pre[j][i] = *(const LAS v4u*)(tb + ka[i & 3] + (i >> 2) * 256); pre[j][8 + i] = *(const LAS v4u*)(tb + ka[i & 3] + (i >> 2) * 256 + 8192); }
        LDSW();
        ctl[4 + w] = (unsigned)(j + 1 < NPRE ? t + 3 : 3 * NPRE + w);
        PIN16(pre[j]);
      }
      WAIT(RFL(ctl[10]) == 0u, 2) { __builtin_amdgcn_s_sleep(2); }
      asm volatile("" ::: "memory");
      tag = RFL(ctl[3]);
    }
    v4u qf[8];
#pragma unroll
    for (int i = 0; i < 8; ++i) qf[i] = *(const LAS v4u*)(L + Q_OFF + head * 512 + 64 * i + 16 * g);
    float mx = -INFINITY;
    auto qk = [&](const v4u (&a)[16], int t) {
      f32x4 d0 = {0.f, 0.f, 0.f, 0.f}, d1 = {0.f, 0.f, 0.f, 0.f};
#pragma unroll
      for (int i = 0; i < 8; ++i) { d0 = mfma(a[i], qf[i], d0); d1 = mfma(a[8 + i], qf[i], d1); }
      mx = fmaxf(mx, fmaxf(fmaxf(fmaxf(d0[0], d0[1]), fmaxf(d0[2], d0[3])), fmaxf(fmaxf(d1[0], d1[1]), fmaxf(d1[2], d1[3]))));
      if (r16 < 4) {
        const f32x4 d = (r16 & 2) ? d1 : d0;
        *(LAS f32x4*)(L + S_OFF + head * (KPS * 4) + (t * 32 + (r16 & 2) * 8 + 4 * g) * 4) = d;
      }
    };
    if (w > 0) {
#pragma unroll
      for (int j = 0; j < NPRE; ++j) qk(pre[j], (w - 1) + 3 * j);
    }
#pragma nounroll
    for (int t = 3 * NPRE + w; t < NKT; t += NCONS) {
      wait_full(t + 1);
      LAS unsigned char* tb = L + (t & (NSLOT - 1)) * TILE_B;
      v4u a[16];
#pragma unroll
      for (int i = 0; i < 8; ++i) { a[i] = *(const LAS v4u*)(tb + ka[i & 3] + (i >> 2) * 256); a[8 + i] = *(const LAS v4u*)(tb + ka[i & 3] + (i >> 2) * 256 + 8192); }
      LDSW();
      ctl[4 + w] = (unsigned)(t + NCONS < NKT ? t + NCONS : NKT);
      PIN16(a);
      qk(a, t);
    }
    mx = x16max(mx);
    if (lane < 2) ((LAS float*)(L + C_OFF))[16 + w * 2 + lane] = mx;
    LDSW();
    if (lane == 0) __hip_atomic_fetch_add((LAS unsigned*)(L + C_OFF) + 8, 1u, __ATOMIC_RELAXED, __HIP_MEMORY_SCOPE_WORKGROUP);
    WAIT(RFL(ctl[8]) < NCONS, 1) { __builtin_amdgcn_s_sleep(1); }
    asm volatile("" ::: "memory");
    constexpr float C2 = 0.0625f * 1.4426950408889634f;
    {
      const f32x4 ma = *(const LAS f32x4*)(L + C_OFF + 64), mb = *(const LAS f32x4*)(L + C_OFF + 80);
      const float M0 = fmaxf(fmaxf(ma[0], ma[2]), fmaxf(mb[0], mb[2])) * C2, M1 = fmaxf(fmaxf(ma[1], ma[3]), fmaxf(mb[1], mb[3])) * C2;
      const int tt = w * 64 + lane, tile = tt >> 2, gg = tt & 3;
      float sum[2];
#pragma unroll
      for (int h = 0; h < 2; ++h) {
        const f32x4 sa = *(const LAS f32x4*)(L + S_OFF + h * (KPS * 4) + (tile * 32 + 4 * gg) * 4);
        const f32x4 sb = *(const LAS f32x4*)(L + S_OFF + h * (KPS * 4) + (tile * 32 + 16 + 4 * gg) * 4);
        const float mc = h ? M1 : M0;
        float p[8];
#pragma unroll
        for (int j = 0; j < 4; ++j) { p[j] = __builtin_amdgcn_exp2f(fmaf(sa[j], C2, -mc)); p[4 + j] = __builtin_amdgcn_exp2f(fmaf(sb[j], C2, -mc)); }
        sum[h] = ((p[0] + p[1]) + (p[2] + p[3])) + ((p[4] + p[5]) + (p[6] + p[7]));
        v4u pk = {cvtpk(p[0], p[1]), cvtpk(p[2], p[3]), cvtpk(p[4], p[5]), cvtpk(p[6], p[7])};
        *(LAS v4u*)(L + P_OFF + tile * 128 + gg * 32 + h * 16) = pk;
      }
      sum[0] = wavesum(sum[0]); sum[1] = wavesum(sum[1]);
      if (lane < 2) ((LAS float*)(L + C_OFF))[24 + w * 2 + lane] = lane ? sum[1] : sum[0];
      LDSW();
      if (lane == 0) __hip_atomic_fetch_add((LAS unsigned*)(L + C_OFF) + 9, 1u, __ATOMIC_RELAXED, __HIP_MEMORY_SCOPE_WORKGROUP);
      WAIT(RFL(ctl[9]) < NCONS, 1) { __builtin_amdgcn_s_sleep(1); }
      asm volatile("" ::: "memory");
      if (w == 1 && lane < 2) {
        const f32x4 la = *(const LAS f32x4*)(L + C_OFF + 96), lb = *(const LAS f32x4*)(L + C_OFF + 112);
        const float Ls = lane ? ((la[1] + la[3]) + (lb[1] + lb[3])) : ((la[0] + la[2]) + (lb[0] + lb[2]));
        const v4u gr = {__float_as_uint(lane ? M1 : M0), tag, __float_as_uint(Ls), tag};
        ST_SC1(gr, WS_ST + ((b * 16 + kvh * 2 + lane) * 4 + s) * 16);
      }
    }
    const int key = 4 * g + qq;
    unsigned va[4];
#pragma unroll
    for (int u = 0; u < 4; ++u) va[u] = key * 512 + (((8 * w + 2 * u + (pp >> 1)) ^ (2 * (key & 7))) << 4) + (pp & 1) * 8;
    const unsigned pa = P_OFF + g * 32 + head * 16;
#pragma nounroll
    for (int t = 0; t < NKT; ++t) {
      if (t == ST_AT) {
        v4u sc = {0u, 0u, 0u, 0u};
        WAIT(true, 16) {
          sc = LD_SC1(WS_ST + (b * 16 + kvh * 2) * 64 + (lane & 7) * 16);
          if (__all(sc.y == tag && sc.w == tag)) break;
          __builtin_amdgcn_s_sleep(8);
          asm volatile("" ::: "memory");
        }
        const float ms = __uint_as_float(sc.x), ls = __uint_as_float(sc.z);
        float M = fmaxf(ms, dpp<XOR1>(ms)); M = fmaxf(M, dpp<XOR2>(M));
        const float e = __builtin_amdgcn_exp2f(ms - M);
        float Lt = e * ls; Lt += dpp<XOR1>(Lt); Lt += dpp<XOR2>(Lt);
        const int wgt = __builtin_bit_cast(int, e / Lt);
        const int f0 = __builtin_amdgcn_readlane(wgt, s), f1 = __builtin_amdgcn_readlane(wgt, 4 + s);
        oscale = __builtin_bit_cast(float, head ? f1 : f0);
      }
      v4u pf = *(const LAS v4u*)(L + pa + t * 128);
      if (t == NKT - 1) tgate = RFL(ctl[13]) + ((NT * TAU) >> 4) + G1;
      wait_full(NKT + t + 1);
      LAS unsigned char* tb = L + (t & (NSLOT - 1)) * TILE_B;
      v4u av[4];
#pragma unroll
      for (int u = 0; u < 4; ++u) { const v2u lo = trread(tb + va[u]), hi = trread(tb + va[u] + 8192); av[u] = v4u{lo.x, lo.y, hi.x, hi.y}; }
      LDSW();
      ctl[4 + w] = (unsigned)(NKT + t + 1);
      asm volatile("" : "+v"(av[0]), "+v"(av[1]), "+v"(av[2]), "+v"(av[3]), "+v"(pf) :: "memory");
#pragma unroll
      for (int u = 0; u < 4; ++u) acc[u] = mfma(av[u], pf, acc[u]);
    }
  }
  STAMP(5 + w);
  {
    const int u = (r16 >> 1) & 3;
    const f32x4 a01 = (u & 1) ? acc[1] : acc[0], a23 = (u & 1) ? acc[3] : acc[2];
    const f32x4 a = ((u & 2) ? a23 : a01) * oscale;
    const v4u o = {cvtpk(a[0], a[1]), tag, cvtpk(a[2], a[3]), tag};
    const int off = WS_PO + ((b * 16 + kvh * 2 + head) * 4 + s) * 1024 + (64 * w + 16 * u + 4 * g) * 4;
    ST_SC1(o, (r16 < 8 && live) ? off : WS_BYTES);
  }
  if (!has_out) { STAMP_FLUSH(); return; }
  const u64 wsa = (u64)ws;
  const v4i srd = {(int)(unsigned)wsa, (int)((unsigned)(wsa >> 32) & 0xffffu), WS_BYTES, 0x00020000};
  v4u p0, p1, p2, p3;
  auto gather_issue = [&](int voff) {
    asm volatile("buffer_load_dwordx4 %0, %4, %5, 0 offen sc1\n\t"
                 "buffer_load_dwordx4 %1, %4, %5, 0 offen offset:1024 sc1\n\t"
                 "buffer_load_dwordx4 %2, %4, %5, 0 offen offset:2048 sc1\n\t"
                 "buffer_load_dwordx4 %3, %4, %5, 0 offen offset:3072 sc1"
                 : "=&v"(p0), "=&v"(p1), "=&v"(p2), "=&v"(p3) : "v"(voff), "s"(srd) : "memory");
  };
  auto gather_wait = [&](int voff) {
    int sp = 0;
    WAIT(true, 16) {
      asm volatile("s_waitcnt vmcnt(0)" : "+v"(p0), "+v"(p1), "+v"(p2), "+v"(p3) :: "memory");
      const unsigned want = tag;
      const u64 m0 = __builtin_amdgcn_ballot_w64(p0.y != want || p0.w != want), m1 = __builtin_amdgcn_ballot_w64(p1.y != want || p1.w != want);
      const u64 m2 = __builtin_amdgcn_ballot_w64(p2.y != want || p2.w != want), m3 = __builtin_amdgcn_ballot_w64(p3.y != want || p3.w != want);
      if (!(m0 | m1 | m2 | m3)) break;
      if (++sp > 16) __builtin_amdgcn_s_sleep(8);
      asm volatile("s_mov_b64 exec, %5\n\tbuffer_load_dwordx4 %0, %4, %9, 0 offen sc1\n\t"
                   "s_mov_b64 exec, %6\n\tbuffer_load_dwordx4 %1, %4, %9, 0 offen offset:1024 sc1\n\t"
                   "s_mov_b64 exec, %7\n\tbuffer_load_dwordx4 %2, %4, %9, 0 offen offset:2048 sc1\n\t"
                   "s_mov_b64 exec, %8\n\tbuffer_load_dwordx4 %3, %4, %9, 0 offen offset:3072 sc1\n\t"
                   "s_mov_b64 exec, -1"
                   : "+v"(p0), "+v"(p1), "+v"(p2), "+v"(p3) : "v"(voff), "s"(m0), "s"(m1), "s"(m2), "s"(m3), "s"(srd) : "memory");
    }
    asm volatile("s_waitcnt vmcnt(0)" : "+v"(p0), "+v"(p1), "+v"(p2), "+v"(p3) :: "memory");
  };
  {
    const int po = WS_PO + ((lane >> 3) * 16 + (rg >> 1)) * 4096 + ((rg & 1) * 128 + 32 * w) * 4 + (lane & 7) * 16;
    SPIN sp < 32 && (int)(now() - tgate) < 0; ++sp) __builtin_amdgcn_s_sleep(1);
    gather_issue(po);
    WAIT(RFL(ctl[11]) == 0u, 1) { __builtin_amdgcn_s_sleep(1); }
    asm volatile("" ::: "memory");
    v4u wa[4];
    {
      LAS unsigned char* wb = L + S_OFF + (8 * g + qq) * 128 + w * 32 + pp * 8;
#pragma unroll
      for (int k = 0; k < 4; ++k) { const v2u lo = trread(wb + k * 4096), hi = trread(wb + k * 4096 + 512); wa[k] = v4u{lo.x, lo.y, hi.x, hi.y}; }
    }
    const int yo = WS_Y2 + ((((g & 1) * 4 + (g >> 1)) * 7 + lc) * 4 + w) * 4096 + rg * 128 + r16 * 8;
    gather_wait(po);
    STAMP(9 + w);
    LAS unsigned char* xb = L + X_OFF + w * 2048 + lane * 8;
    *(LAS v2u*)(xb) = v2u{p0.x, p0.z}; *(LAS v2u*)(xb + 512) = v2u{p1.x, p1.z};
    *(LAS v2u*)(xb + 1024) = v2u{p2.x, p2.z}; *(LAS v2u*)(xb + 1536) = v2u{p3.x, p3.z};
    LDSW();
    __builtin_amdgcn_s_barrier();
    asm volatile("" ::: "memory");
    v4u oa[8];
#pragma unroll
    for (int k = 0; k < 8; ++k) oa[k] = *(const LAS v4u*)(L + X_OFF + k * 1024 + r16 * 64 + g * 16);
    f32x4 ya = {0.f, 0.f, 0.f, 0.f}, yb = {0.f, 0.f, 0.f, 0.f};
#pragma unroll
    for (int k = 0; k < 4; ++k) { ya = mfma(oa[2 * k], wa[k], ya); yb = mfma(oa[2 * k + 1], wa[k], yb); }
    ya += yb;
    auto t0 = __builtin_amdgcn_permlane32_swap(__float_as_uint(ya[0]), __float_as_uint(ya[1]), false, false);
    auto t1 = __builtin_amdgcn_permlane32_swap(__float_as_uint(ya[2]), __float_as_uint(ya[3]), false, false);
    const float v0 = __uint_as_float(t0[0]) + __uint_as_float(t0[1]), v1 = __uint_as_float(t1[0]) + __uint_as_float(t1[1]);
    __builtin_amdgcn_raw_buffer_store_b64(v2u{__float_as_uint(v0), tag}, rs, yo, 0, 16);
    __builtin_amdgcn_raw_buffer_store_b64(v2u{__float_as_uint(v1), tag}, rs, yo + 2 * 7 * 4 * 4096, 0, 16);
  }
  if (w >= S3W) { STAMP_FLUSH(); return; }
  STAMP(14);
  {
    const int nt = (bid >> 3) & 3;
    const int yo = WS_Y2 + ((b3 * 7 + lc) * 4 + nt) * 4096 + lane * 16;
    __builtin_amdgcn_s_sleep(D2);
    if (w & 1) __builtin_amdgcn_s_sleep(S3D);
    if (w & 2) __builtin_amdgcn_s_sleep(2 * S3D);
    gather_issue(yo);
    gather_wait(yo);
    STAMP(15);
    float v0 = (__uint_as_float(p0.x) + __uint_as_float(p1.x)) + (__uint_as_float(p2.x) + __uint_as_float(p3.x));
    float v1 = (__uint_as_float(p0.z) + __uint_as_float(p1.z)) + (__uint_as_float(p2.z) + __uint_as_float(p3.z));
    v0 += dpp<XOR8>(v0); v1 += dpp<XOR8>(v1);
    v0 = swap32add(swap16add(v0)); v1 = swap32add(swap16add(v1));
    const __amdgpu_buffer_rsrc_t ro = __builtin_amdgcn_make_buffer_rsrc((void*)out, 0, B * D * 2, 0x00020000);
    __builtin_amdgcn_raw_buffer_store_b32(cvtpk(v0, v1), ro, (lane < 8 && live) ? (b3 * D + lc * 64 + nt * 16 + 2 * lane) * 2 : B * D * 2, 0, OSC);
    __builtin_amdgcn_raw_buffer_store_b32(tag, rs, (bid == 0 && lane == 0 && live) ? WS_E : WS_BYTES, 0, 16);
    STAMP(13);
    STAMP_FLUSH();
  }
}

extern "C" void kernel_launch(void* const* d_in, const int* in_sizes, int n_in, void* d_out, int out_size, void* d_ws, size_t ws_size, hipStream_t stream) {
  (void)hipFuncSetAttribute((const void*)k_fused, hipFuncAttributeMaxDynamicSharedMemorySize, LDS_BYTES);
  k_fused<<<B * HKV * NSPLIT, NWAVE * 64, LDS_BYTES, stream>>>((const unsigned short*)d_in[0], (const unsigned short*)d_in[1], (const unsigned short*)d_in[2],
      (const unsigned short*)d_in[3], (const unsigned short*)d_in[4], (unsigned short*)d_out, (unsigned char*)d_ws);
}
```

```cpp
#include <hip/hip_runtime.h>
#include <hip/hip_bf16.h>
#include <stdint.h>

typedef unsigned v4u __attribute__((ext_vector_type(4)));
typedef unsigned v2u __attribute__((ext_vector_type(2)));
typedef int v4i __attribute__((ext_vector_type(4)));
typedef float f32x4 __attribute__((ext_vector_type(4)));
typedef float f32x2 __attribute__((ext_vector_type(2)));
typedef short s16x4 __attribute__((ext_vector_type(4)));
typedef __bf16 bf16x8 __attribute__((ext_vector_type(8)));
typedef __bf16 bf16x2 __attribute__((ext_vector_type(2)));
typedef unsigned long long u64;
#define LAS __attribute__((address_space(3)))
#define GAS __attribute__((address_space(1)))
#define RLX_AGENT __ATOMIC_RELAXED, __HIP_MEMORY_SCOPE_AGENT
#define RLX_WG __ATOMIC_RELAXED, __HIP_MEMORY_SCOPE_WORKGROUP

constexpr int B = 8, D = 448, DH = 256;
constexpr int KPS = 2048;
#define TAU 880
#define LEAD 30
#define TH1 40
#define TH2 150
#define PK0 8
#define PKG 24
#define PKD 3
#define PFD 32
#define QD 550
#define WO_AT 127
#define G1 100
#define D2 15
constexpr int NPRE = 3;
constexpr int TILE_B = 16384, NSLOT = 8, NKT = KPS / 32, NT = 2 * NKT;
constexpr int NCONS = 4, NWAVE = 6;
constexpr int WAIT_BUDGET = 1 << 22;
constexpr int S_OFF = NSLOT * TILE_B;
constexpr int X_OFF = S_OFF + 2 * KPS * 4;
constexpr int P_OFF = X_OFF + 2048;
constexpr int Q_OFF = P_OFF + NKT * 128;
constexpr int C_OFF = Q_OFF + 1024;
constexpr int LDS_BYTES = C_OFF + 256;
constexpr int WS_E = 0;
constexpr int WS_QG = 4096;
constexpr int WS_ST = WS_QG + 8 * 2048 * 8;
constexpr int WS_PO = WS_ST + 16 * 8 * 4 * 16;
constexpr int WS_Y2 = WS_PO + 8 * 16 * 4 * 1024;
constexpr int WS_PH = WS_Y2 + 8 * 7 * 4 * 4096;
constexpr int WS_BYTES = WS_PH + 256 * 128;

__device__ __forceinline__ unsigned cvtpk(float lo, float hi) { f32x2 v = {lo, hi}; return __builtin_bit_cast(unsigned, __builtin_convertvector(v, bf16x2)); }
__device__ __forceinline__ f32x4 mfma(v4u a, v4u b, f32x4 c) {
  return __builtin_amdgcn_mfma_f32_16x16x32_bf16(__builtin_bit_cast(bf16x8, a), __builtin_bit_cast(bf16x8, b), c, 0, 0, 0);
}
__device__ __forceinline__ v2u trread(LAS unsigned char* p) { return __builtin_bit_cast(v2u, __builtin_amdgcn_ds_read_tr16_b64_v4i16((LAS s16x4*)p)); }
__device__ __forceinline__ float x16max(float x) {
  auto s = __builtin_amdgcn_permlane16_swap(__float_as_uint(x), __float_as_uint(x), false, false);
  x = fmaxf(__uint_as_float(s[0]), __uint_as_float(s[1]));
  auto t = __builtin_amdgcn_permlane32_swap(__float_as_uint(x), __float_as_uint(x), false, false);
  return fmaxf(__uint_as_float(t[0]), __uint_as_float(t[1]));
}
__device__ __forceinline__ float x16sum(float x) {
  auto s = __builtin_amdgcn_permlane16_swap(__float_as_uint(x), __float_as_uint(x), false, false);
  x = __uint_as_float(s[0]) + __uint_as_float(s[1]);
  auto t = __builtin_amdgcn_permlane32_swap(__float_as_uint(x), __float_as_uint(x), false, false);
  return __uint_as_float(t[0]) + __uint_as_float(t[1]);
}
template <int CTRL> __device__ __forceinline__ float dpp(float x) {
  return __builtin_bit_cast(float, __builtin_amdgcn_mov_dpp(__builtin_bit_cast(int, x), CTRL, 0xf, 0xf, true));
}
constexpr int XOR1 = 0xB1, XOR2 = 0x4E, XOR7 = 0x141, XOR8 = 0x128;
__device__ __forceinline__ float wavesum(float x) {
  x += dpp<XOR1>(x); x += dpp<XOR2>(x); x += dpp<XOR7>(x); x += dpp<XOR8>(x);
  return x16sum(x);
}
__device__ __forceinline__ unsigned now() { return (unsigned)__builtin_amdgcn_s_memrealtime(); }
#define LDSW() asm volatile("s_waitcnt lgkmcnt(0)" ::: "memory")
#define DRAIN() asm volatile("s_waitcnt vmcnt(0)" ::: "memory")
#define FENCE() asm volatile("" ::: "memory")
#define PIN16(a) asm volatile("" : "+v"(a[0]), "+v"(a[1]), "+v"(a[2]), "+v"(a[3]), "+v"(a[4]), "+v"(a[5]), "+v"(a[6]), "+v"(a[7]), \
                        "+v"(a[8]), "+v"(a[9]), "+v"(a[10]), "+v"(a[11]), "+v"(a[12]), "+v"(a[13]), "+v"(a[14]), "+v"(a[15]) :: "memory")
#define RFL(x) __builtin_amdgcn_readfirstlane(x)
#define SLEEP(n) __builtin_amdgcn_s_sleep(n)
#define LD_SC1(off) __builtin_amdgcn_raw_buffer_load_b128(rs, (off), 0, 16)
#define ST_SC1(v, off) __builtin_amdgcn_raw_buffer_store_b128((v), rs, (off), 0, 16)
#define SPIN _Pragma("nounroll") for (int sp = 0;
#define WAIT(cond, cost) _Pragma("nounroll") for (; bud > 0 && (cond); bud -= (cost))

__global__ void __launch_bounds__(NWAVE * 64, 1) k_fused(const unsigned short* __restrict__ x, const unsigned short* __restrict__ kc, const unsigned short* __restrict__ vc,
                                              const unsigned short* __restrict__ wq, const unsigned short* __restrict__ wo, unsigned short* __restrict__ out, unsigned char* ws) {
  extern __shared__ __attribute__((aligned(16))) unsigned char lds[];
  LAS unsigned char* L = (LAS unsigned char*)lds;
  volatile LAS unsigned* ctl = (volatile LAS unsigned*)(L + C_OFF);
  const int bid = blockIdx.x;
  const int lane = threadIdx.x & 63, wave = RFL(threadIdx.x >> 6);
  int bud = WAIT_BUDGET;
  const unsigned o0 = (lane >> 5) * 512 + (((lane & 31) ^ ((lane >> 5) << 1)) << 4);
  const unsigned o1 = o0 ^ 64, o2 = o0 ^ 128, o3 = o0 ^ 192;
  const unsigned ldsbase = (unsigned)(size_t)L;
  u64 src = (u64)kc + (u64)bid * (KPS * DH * 2);
#define DMA4(S) "global_load_lds_dwordx4 %1, " S " nt\n\tglobal_load_lds_dwordx4 %2, " S " offset:1024 nt\n\t" \
                "global_load_lds_dwordx4 %3, " S " offset:2048 nt\n\tglobal_load_lds_dwordx4 %4, " S " offset:3072 nt\n\t"
  {
    unsigned keep, cnt, c0 = o0, c1 = o1, c2 = o2, c3 = o3;
    asm volatile("s_cmp_lg_u32 %8, 0\n\ts_cbranch_scc1 .Lskip%=\n\t"
                 "s_setprio 3\n\t"
                 "s_mov_b32 %0, m0\n\ts_mov_b32 m0, %7\n\ts_mov_b32 %5, 8\n"
                 ".Lloop%=:\n\t" DMA4("%6")
                 "s_add_u32 m0, m0, 0x1000\n\t"
                 "v_add_u32 %1, 0x1000, %1\n\tv_add_u32 %2, 0x1000, %2\n\tv_add_u32 %3, 0x1000, %3\n\tv_add_u32 %4, 0x1000, %4\n\t"
                 "s_sub_u32 %5, %5, 1\n\ts_cmp_lg_u32 %5, 0\n\ts_cbranch_scc1 .Lloop%=\n\t"
                 "s_mov_b32 m0, %0\n"
                 ".Lskip%=:"
                 : "=&s"(keep), "+v"(c0), "+v"(c1), "+v"(c2), "+v"(c3), "=&s"(cnt)
                 : "s"(src), "s"(ldsbase), "s"(wave) : "memory", "scc");
  }
  if (threadIdx.x < 64) ((LAS unsigned*)(L + C_OFF))[lane] = 0u;
  LDSW();
  __builtin_amdgcn_s_barrier();
  if (wave == 0) {
    const u64 vb = (u64)vc + (u64)bid * (KPS * DH * 2);
    const unsigned xm = (unsigned)(bid & 1) * TILE_B;
    int pub = 0;
    unsigned phi = 0;
#pragma nounroll
    for (int t = 2; t < NT + 3; ++t) {
      if (t >= NSLOT && t < NT) {
        const unsigned needt = (unsigned)(t - NSLOT);
        WAIT(true, 1) {
          const v4u nd = *(volatile LAS v4u*)(L + C_OFF + 16);
          const unsigned mn = min(min(nd.x, nd.y), min(nd.z, nd.w));
          if (RFL(mn) > needt) break;
          SLEEP(1);
        }
        FENCE();
      }
      const v2u rf = *(volatile LAS v2u*)(L + C_OFF + 48);
      const unsigned rvalid = RFL(rf.x), ref = RFL(rf.y);
      const int e = t >= NT ? (t == NT ? TH2 + 1 : t == NT + 1 ? TH1 + 1 : 0) : rvalid ? (int)(phi - ref) : 0;
      const unsigned tt = ((unsigned)t * TAU) >> 4;
      const int issued = t < NT ? t : NT;
      if (t == NKT) src = vb;
      int landed; unsigned adj;
      if (e > TH2) { asm volatile("s_waitcnt vmcnt(47)" ::: "memory"); landed = issued - 3; adj = (31 * TAU) >> 8; }
      else if (e > TH1) { asm volatile("s_waitcnt vmcnt(32)" ::: "memory"); landed = issued - 2; adj = TAU >> 4; }
      else {
        asm volatile("s_waitcnt vmcnt(16)" ::: "memory"); landed = issued - 1; adj = 0;
        if (e < 0) { const unsigned due = ref + tt - LEAD; SPIN sp < 64 && (int)(now() - due) < 0; ++sp) SLEEP(1); }
      }
      if (t < NT) {
        const unsigned dst = ldsbase + (t & (NSLOT - 1)) * TILE_B;
        const unsigned ti = (unsigned)(t & (NKT - 1));
        const u64 s0 = src + ((ti * TILE_B) ^ (ti < 2 ? 0u : xm));
        const u64 s1 = s0 + 4096, s2 = s0 + 8192, s3 = s0 + 12288;
        unsigned keep;
        asm volatile("s_mov_b32 %0, m0\n\t"
                     "s_mov_b32 m0, %9\n\ts_nop 0\n\t" DMA4("%5")
                     "s_add_u32 m0, %9, 0x1000\n\ts_nop 0\n\t" DMA4("%6")
                     "s_add_u32 m0, %9, 0x2000\n\ts_nop 0\n\t" DMA4("%7")
                     "s_add_u32 m0, %9, 0x3000\n\ts_nop 0\n\t" DMA4("%8")
                     "s_mov_b32 m0, %0"
                     : "=&s"(keep) : "v"(o0), "v"(o1), "v"(o2), "v"(o3), "s"(s0), "s"(s1), "s"(s2), "s"(s3), "s"(dst) : "memory", "scc");
      }
      if (landed > pub) pub = landed;
      phi = now() - tt + adj;
      *(volatile LAS v2u*)(L + C_OFF) = v2u{(unsigned)pub, phi};
    }
    DRAIN();
    ctl[0] = (unsigned)NT;
    return;
  }
  const int s = bid & 3, kvh = (bid >> 2) & 7, b = bid >> 5;
  const bool has_out = (bid >> 5) < 7;
  const int lc = has_out ? (bid >> 5) : 0, rg = (bid & 7) * 4 + ((bid >> 3) & 3);
  auto wait_full = [&](unsigned n) { WAIT(RFL(ctl[0]) < n, 1) { SLEEP(1); } FENCE(); };
  auto wait_full_slow = [&](unsigned n) { WAIT(RFL(ctl[0]) < n, 4) { SLEEP(4); } FENCE(); };
  if (wave == 5) {
    WAIT(RFL(ctl[2]) == 0u, 4) { SLEEP(4); }
    const unsigned tag = RFL(ctl[3]);
    int mixs = 4;
#pragma nounroll
    for (int k = PK0; k + PKD < WO_AT; k += PKG) {
      wait_full_slow(k);
      const unsigned mine = RFL(ctl[1]);
      if (lane == 0) __hip_atomic_store((GAS u64*)(ws + WS_PH) + bid * 16, ((u64)tag << 32) | mine, RLX_AGENT);
      wait_full_slow(k + PKD);
      u64 o = 0;
      if (lane < 8) o = __hip_atomic_load((GAS u64*)(ws + WS_PH) + (((((bid >> 3) + mixs * lane) & 31) << 3) + lane) * 16, RLX_AGENT);
      mixs = 5 - mixs;
      const bool ok = (unsigned)(o >> 32) == tag && lane < 8;
      const float dsum = wavesum(ok ? (float)(int)((unsigned)o - mine) : 0.f);
      const int n = __builtin_popcountll(__builtin_amdgcn_ballot_w64(ok));
      if (n >= 4) { const unsigned ref = mine + (unsigned)(int)(dsum / (float)n); *(volatile LAS v2u*)(L + C_OFF + 48) = v2u{1u, ref}; }
    }
    if (!has_out) return;
    const unsigned char* wsrc = (const unsigned char*)wo + (size_t)(rg * 128 + (lane >> 3)) * (D * 2) + lc * 128 + (lane & 7) * 16;
    wait_full(WO_AT);
    WAIT(RFL(ctl[9]) < NCONS, 4) { SLEEP(4); }
    FENCE();
#pragma unroll
    for (int m = 0; m < 16; ++m) __builtin_amdgcn_global_load_lds((const GAS unsigned*)(wsrc + m * 8 * (D * 2)), (LAS unsigned*)(L + S_OFF + m * 1024), 16, 0, 2);
    DRAIN();
    ctl[11] = 1u;
    return;
  }
  const __amdgpu_buffer_rsrc_t rs = __builtin_amdgcn_make_buffer_rsrc((void*)ws, 0, WS_BYTES, 0x00020000);
  const int w = wave - 1;
  const int g = lane >> 4, r16 = lane & 15, head = lane & 1;
  const int qq = (lane >> 2) & 3, pp = lane & 3;
  unsigned tag;
  float oscale;
  {
    unsigned ka[4];
#pragma unroll
    for (int i = 0; i < 4; ++i) ka[i] = r16 * 512 + (((4 * i + g) ^ (2 * (r16 & 7))) << 4);
    v4u pre[NPRE][16];
    if (w == 0) {
      const unsigned tq0 = now();
      ctl[4] = (unsigned)(3 * NPRE);
      unsigned eraw = __hip_atomic_load((GAS unsigned*)(ws + WS_E), RLX_AGENT);
      const int ctile = (bid & 7) * 32 + (bid >> 3);
      {
        const unsigned short* ps = wq + (size_t)((bid >> 3) * 14) * 4096 + (bid & 7) * 512 + lane * 8;
#pragma unroll
        for (int j = 0; j < 14; ++j) __builtin_amdgcn_global_load_lds((const GAS unsigned*)(ps + (size_t)j * 4096), (LAS unsigned*)(L + S_OFF + j * 1024), 16, 0, 0);
        DRAIN();
        SLEEP(PFD);
      }
      const unsigned short* wsrc = wq + (size_t)(lane >> 1) * 4096 + ctile * 16 + (lane & 1) * 8;
#pragma unroll
      for (int j = 0; j < 14; ++j) __builtin_amdgcn_global_load_lds((const GAS unsigned*)(wsrc + (size_t)j * 32 * 4096), (LAS unsigned*)(L + S_OFF + j * 1024), 16, 0, 2);
#pragma unroll
      for (int j = 0; j < 7; ++j) __builtin_amdgcn_global_load_lds((const GAS unsigned*)(x + j * 512 + lane * 8), (LAS unsigned*)(L + X_OFF + j * 1024), 16, 0, 0);
      __builtin_amdgcn_sched_barrier(0);
      DRAIN();
      asm volatile("" : "+v"(eraw));
      tag = RFL(eraw) + 1u;
      *(volatile LAS v2u*)(L + C_OFF + 8) = v2u{1u, tag};
      v4u xf[14], wf[14];
      LAS unsigned char* wb = L + S_OFF + (8 * g + qq) * 32 + pp * 8;
#pragma unroll
      for (int j = 0; j < 14; ++j) {
        xf[j] = *(const LAS v4u*)(L + X_OFF + (lane & 7) * (D * 2) + 64 * j + 16 * g);
        const v2u lo = trread(wb + j * 1024), hi = trread(wb + j * 1024 + 128); wf[j] = v4u{lo.x, lo.y, hi.x, hi.y};
      }
      __builtin_amdgcn_sched_barrier(0);
      f32x4 d = {0.f, 0.f, 0.f, 0.f}, d2 = {0.f, 0.f, 0.f, 0.f};
#pragma unroll
      for (int j = 0; j < 14; j += 2) { d = mfma(wf[j], xf[j], d); d2 = mfma(wf[j + 1], xf[j + 1], d2); }
      d += d2;
      ST_SC1((v4u{cvtpk(d[0], d[1]), tag, cvtpk(d[2], d[3]), tag}), r16 < 8 ? WS_QG + (r16 * 2048 + ctile * 8 + 2 * g) * 8 : WS_BYTES);
      const int qo = WS_QG + (b * 2048 + kvh * 256) * 8 + lane * 16;
      v4u ga = {0u, 0u, 0u, 0u}, gb = ga;
      SPIN sp < 1024 && (int)(now() - tq0) < QD; ++sp) SLEEP(2);
      WAIT(true, 16) {
        ga = LD_SC1(qo);
        gb = LD_SC1(qo + 1024);
        if (__all(ga.y == tag && ga.w == tag && gb.y == tag && gb.w == tag)) break;
        SLEEP(8);
        FENCE();
      }
      *(LAS v2u*)(L + Q_OFF + lane * 8) = v2u{ga.x, ga.z};
      *(LAS v2u*)(L + Q_OFF + 512 + lane * 8) = v2u{gb.x, gb.z};
      LDSW();
      ctl[10] = 1u;
    } else {
#pragma unroll
      for (int j = 0; j < NPRE; ++j) {
        const int t = (w - 1) + 3 * j;
        wait_full(t + 1);
        LAS unsigned char* tb = L + (t & (NSLOT - 1)) * TILE_B;
#pragma unroll
        for (int i = 0; i < 8; ++i) { pre[j][i] = *(const LAS v4u*)(tb + ka[i & 3] + (i >> 2) * 256); pre[j][8 + i] = *(const LAS v4u*)(tb + ka[i & 3] + (i >> 2) * 256 + 8192); }
        LDSW();
        ctl[4 + w] = (unsigned)(j + 1 < NPRE ? t + 3 : 3 * NPRE + w);
        PIN16(pre[j]);
      }
      WAIT(RFL(ctl[10]) == 0u, 2) { SLEEP(2); }
      FENCE();
      tag = RFL(ctl[3]);
    }
    v4u qf[8];
#pragma unroll
    for (int i = 0; i < 8; ++i) qf[i] = *(const LAS v4u*)(L + Q_OFF + head * 512 + 64 * i + 16 * g);
    float mx = -INFINITY;
    auto qk = [&](const v4u (&a)[16], int t) {
      f32x4 d0 = {0.f, 0.f, 0.f, 0.f}, d1 = {0.f, 0.f, 0.f, 0.f};
#pragma unroll
      for (int i = 0; i < 8; ++i) { d0 = mfma(a[i], qf[i], d0); d1 = mfma(a[8 + i], qf[i], d1); }
      mx = fmaxf(mx, fmaxf(fmaxf(fmaxf(d0[0], d0[1]), fmaxf(d0[2], d0[3])), fmaxf(fmaxf(d1[0], d1[1]), fmaxf(d1[2], d1[3]))));
      if (r16 < 4) {
        const f32x4 d = (r16 & 2) ? d1 : d0;
        *(LAS f32x4*)(L + S_OFF + head * (KPS * 4) + (t * 32 + (r16 & 2) * 8 + 4 * g) * 4) = d;
      }
    };
    if (w > 0) {
#pragma unroll
      for (int j = 0; j < NPRE; ++j) qk(pre[j], (w - 1) + 3 * j);
    }
#pragma nounroll
    for (int t = 3 * NPRE + w; t < NKT; t += NCONS) {
      wait_full(t + 1);
      LAS unsigned char* tb = L + (t & (NSLOT - 1)) * TILE_B;
      v4u a[16];
#pragma unroll
      for (int i = 0; i < 8; ++i) { a[i] = *(const LAS v4u*)(tb + ka[i & 3] + (i >> 2) * 256); a[8 + i] = *(const LAS v4u*)(tb + ka[i & 3] + (i >> 2) * 256 + 8192); }
      LDSW();
      ctl[4 + w] = (unsigned)(t + NCONS < NKT ? t + NCONS : NKT);
      PIN16(a);
      qk(a, t);
    }
    mx = x16max(mx);
    if (lane < 2) ((LAS float*)(L + C_OFF))[16 + w * 2 + lane] = mx;
    LDSW();
    if (lane == 0) __hip_atomic_fetch_add((LAS unsigned*)(L + C_OFF) + 8, 1u, RLX_WG);
    WAIT(RFL(ctl[8]) < NCONS, 1) { SLEEP(1); }
    FENCE();
    constexpr float C2 = 0.0625f * 1.4426950408889634f;
    const f32x4 ma = *(const LAS f32x4*)(L + C_OFF + 64), mb = *(const LAS f32x4*)(L + C_OFF + 80);
    const float M0 = fmaxf(fmaxf(ma[0], ma[2]), fmaxf(mb[0], mb[2])) * C2, M1 = fmaxf(fmaxf(ma[1], ma[3]), fmaxf(mb[1], mb[3])) * C2;
    const int tt = w * 64 + lane, tile = tt >> 2, gg = tt & 3;
    float sum[2];
#pragma unroll
    for (int h = 0; h < 2; ++h) {
      const f32x4 sa = *(const LAS f32x4*)(L + S_OFF + h * (KPS * 4) + (tile * 32 + 4 * gg) * 4);
      const f32x4 sb = *(const LAS f32x4*)(L + S_OFF + h * (KPS * 4) + (tile * 32 + 16 + 4 * gg) * 4);
      const float mc = h ? M1 : M0;
      float p[8];
#pragma unroll
      for (int j = 0; j < 4; ++j) { p[j] = __builtin_amdgcn_exp2f(fmaf(sa[j], C2, -mc)); p[4 + j] = __builtin_amdgcn_exp2f(fmaf(sb[j], C2, -mc)); }
      sum[h] = ((p[0] + p[1]) + (p[2] + p[3])) + ((p[4] + p[5]) + (p[6] + p[7]));
      *(LAS v4u*)(L + P_OFF + tile * 128 + gg * 32 + h * 16) = v4u{cvtpk(p[0], p[1]), cvtpk(p[2], p[3]), cvtpk(p[4], p[5]), cvtpk(p[6], p[7])};
    }
    sum[0] = wavesum(sum[0]); sum[1] = wavesum(sum[1]);
    if (lane < 2) ((LAS float*)(L + C_OFF))[24 + w * 2 + lane] = lane ? sum[1] : sum[0];
    LDSW();
    if (lane == 0) __hip_atomic_fetch_add((LAS unsigned*)(L + C_OFF) + 9, 1u, RLX_WG);
    WAIT(RFL(ctl[9]) < NCONS, 1) { SLEEP(1); }
    FENCE();
    const f32x4 la = *(const LAS f32x4*)(L + C_OFF + 96), lb = *(const LAS f32x4*)(L + C_OFF + 112);
    const float Ls = head ? ((la[1] + la[3]) + (lb[1] + lb[3])) : ((la[0] + la[2]) + (lb[0] + lb[2]));
    oscale = 1.0f / Ls;
    if (w == 1) ST_SC1((v4u{__float_as_uint(head ? M1 : M0), tag, __float_as_uint(Ls), tag}), lane < 2 ? WS_ST + (((kvh * 2 + head) * 8 + b) * 4 + s) * 16 : WS_BYTES);
  }
  const int u = (r16 >> 1) & 3;
  int ooff = r16 < 8 ? WS_PO + ((b * 16 + kvh * 2 + head) * 4 + s) * 1024 + (64 * w + 16 * u + 4 * g) * 4 : WS_BYTES;
  asm volatile("" : "+v"(ooff), "+v"(oscale));
  f32x4 acc[4];
#pragma unroll
  for (int k = 0; k < 4; ++k) acc[k] = f32x4{0.f, 0.f, 0.f, 0.f};
  unsigned tgate = 0;
  {
    const int key = 4 * g + qq;
    unsigned va[4];
#pragma unroll
    for (int k = 0; k < 4; ++k) va[k] = key * 512 + (((8 * w + 2 * k + (pp >> 1)) ^ (2 * (key & 7))) << 4) + (pp & 1) * 8;
#pragma nounroll
    for (int t = 0; t < NKT; ++t) {
      v4u pf = *(const LAS v4u*)(L + P_OFF + t * 128 + g * 32 + head * 16);
      if (t == NKT - 1) tgate = RFL(ctl[13]) + ((NT * TAU) >> 4) + G1;
      wait_full(NKT + t + 1);
      LAS unsigned char* tb = L + (t & (NSLOT - 1)) * TILE_B;
      v4u av[4];
#pragma unroll
      for (int k = 0; k < 4; ++k) { const v2u lo = trread(tb + va[k]), hi = trread(tb + va[k] + 8192); av[k] = v4u{lo.x, lo.y, hi.x, hi.y}; }
      LDSW();
      ctl[4 + w] = (unsigned)(NKT + t + 1);
      asm volatile("" : "+v"(av[0]), "+v"(av[1]), "+v"(av[2]), "+v"(av[3]), "+v"(pf) :: "memory");
#pragma unroll
      for (int k = 0; k < 4; ++k) acc[k] = mfma(av[k], pf, acc[k]);
    }
  }
  {
    const f32x4 a01 = (u & 1) ? acc[1] : acc[0], a23 = (u & 1) ? acc[3] : acc[2];
    const f32x4 a = ((u & 2) ? a23 : a01) * oscale;
    ST_SC1((v4u{cvtpk(a[0], a[1]), tag, cvtpk(a[2], a[3]), tag}), ooff);
  }
  if (!has_out) return;
  const u64 wsa = (u64)ws;
  const v4i srd = {(int)(unsigned)wsa, (int)((unsigned)(wsa >> 32) & 0xffffu), WS_BYTES, 0x00020000};
  v4u p0, p1, p2, p3;
  auto gather_issue = [&](int voff) {
    asm volatile("buffer_load_dwordx4 %0, %4, %5, 0 offen sc1\n\t"
                 "buffer_load_dwordx4 %1, %4, %5, 0 offen offset:1024 sc1\n\t"
                 "buffer_load_dwordx4 %2, %4, %5, 0 offen offset:2048 sc1\n\t"
                 "buffer_load_dwordx4 %3, %4, %5, 0 offen offset:3072 sc1"
                 : "=&v"(p0), "=&v"(p1), "=&v"(p2), "=&v"(p3) : "v"(voff), "s"(srd) : "memory");
  };
  auto gather_wait = [&](int voff) {
    int sp = 0;
    WAIT(true, 16) {
      asm volatile("s_waitcnt vmcnt(0)" : "+v"(p0), "+v"(p1), "+v"(p2), "+v"(p3) :: "memory");
      const u64 m0 = __builtin_amdgcn_ballot_w64(p0.y != tag || p0.w != tag), m1 = __builtin_amdgcn_ballot_w64(p1.y != tag || p1.w != tag);
      const u64 m2 = __builtin_amdgcn_ballot_w64(p2.y != tag || p2.w != tag), m3 = __builtin_amdgcn_ballot_w64(p3.y != tag || p3.w != tag);
      if (!(m0 | m1 | m2 | m3)) break;
      if (++sp > 16) SLEEP(8);
      asm volatile("s_mov_b64 exec, %5\n\tbuffer_load_dwordx4 %0, %4, %9, 0 offen sc1\n\t"
                   "s_mov_b64 exec, %6\n\tbuffer_load_dwordx4 %1, %4, %9, 0 offen offset:1024 sc1\n\t"
                   "s_mov_b64 exec, %7\n\tbuffer_load_dwordx4 %2, %4, %9, 0 offen offset:2048 sc1\n\t"
                   "s_mov_b64 exec, %8\n\tbuffer_load_dwordx4 %3, %4, %9, 0 offen offset:3072 sc1\n\t"
                   "s_mov_b64 exec, -1"
                   : "+v"(p0), "+v"(p1), "+v"(p2), "+v"(p3) : "v"(voff), "s"(m0), "s"(m1), "s"(m2), "s"(m3), "s"(srd) : "memory");
    }
    asm volatile("s_waitcnt vmcnt(0)" : "+v"(p0), "+v"(p1), "+v"(p2), "+v"(p3) :: "memory");
  };
  {
    int po = WS_PO + ((lane >> 3) * 16 + (rg >> 1)) * 4096 + ((rg & 1) * 128 + 32 * w) * 4 + (lane & 7) * 16;
    const int so = WS_ST + (rg >> 1) * 512 + (lane & 31) * 16;
    v4u sc;
    asm volatile("buffer_load_dwordx4 %0, %2, %3, 0 offen sc1" : "=&v"(sc), "+v"(po) : "v"(so), "s"(srd) : "memory");
    SPIN sp < 32 && (int)(now() - tgate) < 0; ++sp) SLEEP(1);
    gather_issue(po);
    asm volatile("s_waitcnt vmcnt(4)" : "+v"(sc) :: "memory");
    WAIT(!__all(sc.y == tag && sc.w == tag), 16) {
      SLEEP(8);
      asm volatile("buffer_load_dwordx4 %0, %1, %2, 0 offen sc1\n\ts_waitcnt vmcnt(0)" : "=&v"(sc) : "v"(so), "s"(srd) : "memory");
    }
    f32x4 wya, wyb;
    {
      const float ms = __uint_as_float(sc.x), ls = __uint_as_float(sc.z);
      float M = fmaxf(ms, dpp<XOR1>(ms)); M = fmaxf(M, dpp<XOR2>(M));
      const float e = __builtin_amdgcn_exp2f(ms - M) * ls;
      float Lt = e + dpp<XOR1>(e); Lt += dpp<XOR2>(Lt);
      LAS unsigned char* qb = L + Q_OFF + w * 128;
      *(LAS float*)(qb + (lane & 3) * 32 + ((lane >> 2) & 7) * 4) = e / Lt;
      LDSW();
      wya = *(const LAS f32x4*)(qb + g * 16); wyb = *(const LAS f32x4*)(qb + 64 + g * 16);
    }
    WAIT(RFL(ctl[11]) == 0u, 1) { SLEEP(1); }
    FENCE();
    v4u wa[4];
    {
      LAS unsigned char* wb = L + S_OFF + (8 * g + qq) * 128 + w * 32 + pp * 8;
#pragma unroll
      for (int k = 0; k < 4; ++k) { const v2u lo = trread(wb + k * 4096), hi = trread(wb + k * 4096 + 512); wa[k] = v4u{lo.x, lo.y, hi.x, hi.y}; }
    }
    const int yo = WS_Y2 + ((((g & 1) * 4 + (g >> 1)) * 7 + lc) * 4 + w) * 4096 + rg * 128 + r16 * 8;
    gather_wait(po);
    LAS unsigned char* xb = L + X_OFF + w * 2048 + lane * 8;
    *(LAS v2u*)(xb) = v2u{p0.x, p0.z}; *(LAS v2u*)(xb + 512) = v2u{p1.x, p1.z};
    *(LAS v2u*)(xb + 1024) = v2u{p2.x, p2.z}; *(LAS v2u*)(xb + 1536) = v2u{p3.x, p3.z};
    LDSW();
    __builtin_amdgcn_s_barrier();
    FENCE();
    v4u oa[8];
#pragma unroll
    for (int k = 0; k < 8; ++k) oa[k] = *(const LAS v4u*)(L + X_OFF + k * 1024 + r16 * 64 + g * 16);
    f32x4 ya = {0.f, 0.f, 0.f, 0.f}, yb = {0.f, 0.f, 0.f, 0.f};
#pragma unroll
    for (int k = 0; k < 4; ++k) { ya = mfma(oa[2 * k], wa[k], ya); yb = mfma(oa[2 * k + 1], wa[k], yb); }
    ya = ya * wya + yb * wyb;
    auto t0 = __builtin_amdgcn_permlane32_swap(__float_as_uint(ya[0]), __float_as_uint(ya[1]), false, false);
    auto t1 = __builtin_amdgcn_permlane32_swap(__float_as_uint(ya[2]), __float_as_uint(ya[3]), false, false);
    const float v0 = __uint_as_float(t0[0]) + __uint_as_float(t0[1]), v1 = __uint_as_float(t1[0]) + __uint_as_float(t1[1]);
    __builtin_amdgcn_raw_buffer_store_b64(v2u{__float_as_uint(v0), tag}, rs, yo, 0, 16);
    __builtin_amdgcn_raw_buffer_store_b64(v2u{__float_as_uint(v1), tag}, rs, yo + 2 * 7 * 4 * 4096, 0, 16);
  }
  if (w != 0) return;
  {
    const int nt = (bid >> 3) & 3, b3 = bid & 7;
    const int yo = WS_Y2 + ((b3 * 7 + lc) * 4 + nt) * 4096 + lane * 16;
    SLEEP(D2);
    gather_issue(yo);
    gather_wait(yo);
    float v0 = (__uint_as_float(p0.x) + __uint_as_float(p1.x)) + (__uint_as_float(p2.x) + __uint_as_float(p3.x));
    float v1 = (__uint_as_float(p0.z) + __uint_as_float(p1.z)) + (__uint_as_float(p2.z) + __uint_as_float(p3.z));
    v0 += dpp<XOR8>(v0); v1 += dpp<XOR8>(v1);
    v0 = x16sum(v0); v1 = x16sum(v1);
    const __amdgpu_buffer_rsrc_t ro = __builtin_amdgcn_make_buffer_rsrc((void*)out, 0, B * D * 2, 0x00020000);
    __builtin_amdgcn_raw_buffer_store_b32(cvtpk(v0, v1), ro, lane < 8 ? (b3 * D + lc * 64 + nt * 16 + 2 * lane) * 2 : B * D * 2, 0, 0);
    __builtin_amdgcn_raw_buffer_store_b32(tag, rs, (bid == 0 && lane == 0) ? WS_E : WS_BYTES, 0, 16);
  }
}

extern "C" void kernel_launch(void* const* d_in, const int* in_sizes, int n_in, void* d_out, int out_size, void* d_ws, size_t ws_size, hipStream_t stream) {
  (void)hipFuncSetAttribute((const void*)k_fused, hipFuncAttributeMaxDynamicSharedMemorySize, LDS_BYTES);
  k_fused<<<256, NWAVE * 64, LDS_BYTES, stream>>>((const unsigned short*)d_in[0], (const unsigned short*)d_in[1], (const unsigned short*)d_in[2],
      (const unsigned short*)d_in[3], (const unsigned short*)d_in[4], (unsigned short*)d_out, (unsigned char*)d_ws);
}
```

```cpp
#include <hip/hip_runtime.h>
#include <hip/hip_bf16.h>
#include <stdint.h>

typedef unsigned v4u __attribute__((ext_vector_type(4)));
typedef unsigned v2u __attribute__((ext_vector_type(2)));
typedef int v4i __attribute__((ext_vector_type(4)));
typedef float f32x4 __attribute__((ext_vector_type(4)));
typedef float f32x2 __attribute__((ext_vector_type(2)));
typedef short s16x4 __attribute__((ext_vector_type(4)));
typedef __bf16 bf16x8 __attribute__((ext_vector_type(8)));
typedef __bf16 bf16x2 __attribute__((ext_vector_type(2)));
typedef unsigned long long u64;
#define LAS __attribute__((address_space(3)))
#define GAS __attribute__((address_space(1)))
#define RLX_AGENT __ATOMIC_RELAXED, __HIP_MEMORY_SCOPE_AGENT
#define RLX_WG __ATOMIC_RELAXED, __HIP_MEMORY_SCOPE_WORKGROUP

constexpr int B = 8, D = 448, DH = 256;
constexpr int KPS = 2048;
#define TAU 880
#define LEAD 30
#define TH1 40
#define TH2 150
#define PK0 8
#define PKG 24
#define PKD 3
#define PFD 32
#define QD 550
#define WO_AT 127
#define G1 100
#define D2 15
constexpr int NPRE = 3;
constexpr int TILE_B = 16384, NSLOT = 8, NKT = KPS / 32, NT = 2 * NKT;
constexpr int NCONS = 4, NWAVE = 6;
constexpr int WAIT_BUDGET = 1 << 22;
constexpr int S_OFF = NSLOT * TILE_B;
constexpr int X_OFF = S_OFF + 2 * KPS * 4;
constexpr int P_OFF = X_OFF + 2048;
constexpr int Q_OFF = P_OFF + NKT * 128;
constexpr int C_OFF = Q_OFF + 1024;
constexpr int LDS_BYTES = C_OFF + 256;
constexpr int WS_QG = 4096;
constexpr int WS_ST = WS_QG + 8 * 2048 * 8 + 1024;
constexpr int WS_PO = WS_ST + 16 * 8 * 4 * 16 + 1024;
constexpr int WS_Y2 = WS_PO + 8 * 16 * 4 * 1024 + 1024;
constexpr int WS_PH = WS_Y2 + 8 * 7 * 4 * 4096 + 1024;
constexpr int WS_BYTES = WS_PH + 256 * 128 + 1024;

__device__ __forceinline__ unsigned cvtpk(float lo, float hi) { f32x2 v = {lo, hi}; return __builtin_bit_cast(unsigned, __builtin_convertvector(v, bf16x2)); }
__device__ __forceinline__ f32x4 mfma(v4u a, v4u b, f32x4 c) {
  return __builtin_amdgcn_mfma_f32_16x16x32_bf16(__builtin_bit_cast(bf16x8, a), __builtin_bit_cast(bf16x8, b), c, 0, 0, 0);
}
__device__ __forceinline__ v2u trread(LAS unsigned char* p) { return __builtin_bit_cast(v2u, __builtin_amdgcn_ds_read_tr16_b64_v4i16((LAS s16x4*)p)); }
__device__ __forceinline__ float x16max(float x) {
  auto s = __builtin_amdgcn_permlane16_swap(__float_as_uint(x), __float_as_uint(x), false, false);
  x = fmaxf(__uint_as_float(s[0]), __uint_as_float(s[1]));
  auto t = __builtin_amdgcn_permlane32_swap(__float_as_uint(x), __float_as_uint(x), false, false);
  return fmaxf(__uint_as_float(t[0]), __uint_as_float(t[1]));
}
__device__ __forceinline__ float x16sum(float x) {
  auto s = __builtin_amdgcn_permlane16_swap(__float_as_uint(x), __float_as_uint(x), false, false);
  x = __uint_as_float(s[0]) + __uint_as_float(s[1]);
  auto t = __builtin_amdgcn_permlane32_swap(__float_as_uint(x), __float_as_uint(x), false, false);
  return __uint_as_float(t[0]) + __uint_as_float(t[1]);
}
template <int CTRL> __device__ __forceinline__ float dpp(float x) {
  return __builtin_bit_cast(float, __builtin_amdgcn_mov_dpp(__builtin_bit_cast(int, x), CTRL, 0xf, 0xf, true));
}
constexpr int XOR1 = 0xB1, XOR2 = 0x4E, XOR7 = 0x141, XOR8 = 0x128;
__device__ __forceinline__ float wavesum(float x) {
  x += dpp<XOR1>(x); x += dpp<XOR2>(x); x += dpp<XOR7>(x); x += dpp<XOR8>(x);
  return x16sum(x);
}
__device__ __forceinline__ unsigned now() { return (unsigned)__builtin_amdgcn_s_memrealtime(); }
#define LDSW() asm volatile("s_waitcnt lgkmcnt(0)" ::: "memory")
#define DRAIN() asm volatile("s_waitcnt vmcnt(0)" ::: "memory")
#define FENCE() asm volatile("" ::: "memory")
#define PIN16(a) asm volatile("" : "+v"(a[0]), "+v"(a[1]), "+v"(a[2]), "+v"(a[3]), "+v"(a[4]), "+v"(a[5]), "+v"(a[6]), "+v"(a[7]), \
                        "+v"(a[8]), "+v"(a[9]), "+v"(a[10]), "+v"(a[11]), "+v"(a[12]), "+v"(a[13]), "+v"(a[14]), "+v"(a[15]) :: "memory")
#define RFL(x) __builtin_amdgcn_readfirstlane(x)
#define SLEEP(n) __builtin_amdgcn_s_sleep(n)
#define LD_SC1(off) __builtin_amdgcn_raw_buffer_load_b128(rs, (off), 0, 16)
#define ST_SC1(v, off) __builtin_amdgcn_raw_buffer_store_b128((v), rs, (off), 0, 16)
#define SPIN _Pragma("nounroll") for (int sp = 0;
#define WAIT(cond, cost) _Pragma("nounroll") for (; bud > 0 && (cond); bud -= (cost))

__global__ void __launch_bounds__(NWAVE * 64, 1) k_fused(const unsigned short* __restrict__ x, const unsigned short* __restrict__ kc, const unsigned short* __restrict__ vc,
                                              const unsigned short* __restrict__ wq, const unsigned short* __restrict__ wo, unsigned short* __restrict__ out, unsigned char* ws) {
  extern __shared__ __attribute__((aligned(16))) unsigned char lds[];
  LAS unsigned char* L = (LAS unsigned char*)lds;
  volatile LAS unsigned* ctl = (volatile LAS unsigned*)(L + C_OFF);
  const int bid = blockIdx.x;
  const int lane = threadIdx.x & 63, wave = RFL(threadIdx.x >> 6);
  int bud = WAIT_BUDGET;
  const unsigned o0 = (lane >> 5) * 512 + (((lane & 31) ^ ((lane >> 5) << 1)) << 4);
  const unsigned o1 = o0 ^ 64, o2 = o0 ^ 128, o3 = o0 ^ 192;
  const unsigned ldsbase = (unsigned)(size_t)L;
  u64 src = (u64)kc + (u64)bid * (KPS * DH * 2);
#define DMA4(S) "global_load_lds_dwordx4 %1, " S " nt\n\tglobal_load_lds_dwordx4 %2, " S " offset:1024 nt\n\t" \
                "global_load_lds_dwordx4 %3, " S " offset:2048 nt\n\tglobal_load_lds_dwordx4 %4, " S " offset:3072 nt\n\t"
  {
    unsigned keep, cnt, c0 = o0, c1 = o1, c2 = o2, c3 = o3;
    asm volatile("s_cmp_lg_u32 %8, 0\n\ts_cbranch_scc1 .Lskip%=\n\t"
                 "s_setprio 3\n\t"
                 "s_mov_b32 %0, m0\n\ts_mov_b32 m0, %7\n\ts_mov_b32 %5, 8\n"
                 ".Lloop%=:\n\t" DMA4("%6")
                 "s_add_u32 m0, m0, 0x1000\n\t"
                 "v_add_u32 %1, 0x1000, %1\n\tv_add_u32 %2, 0x1000, %2\n\tv_add_u32 %3, 0x1000, %3\n\tv_add_u32 %4, 0x1000, %4\n\t"
                 "s_sub_u32 %5, %5, 1\n\ts_cmp_lg_u32 %5, 0\n\ts_cbranch_scc1 .Lloop%=\n\t"
                 "s_mov_b32 m0, %0\n"
                 ".Lskip%=:"
                 : "=&s"(keep), "+v"(c0), "+v"(c1), "+v"(c2), "+v"(c3), "=&s"(cnt)
                 : "s"(src), "s"(ldsbase), "s"(wave) : "memory", "scc");
  }
  if (threadIdx.x < 64) ((LAS unsigned*)(L + C_OFF))[lane] = 0u;
  LDSW();
  __builtin_amdgcn_s_barrier();
  if (wave == 0) {
    const u64 vb = (u64)vc + (u64)bid * (KPS * DH * 2);
    const unsigned xm = (unsigned)(bid & 1) * TILE_B;
    int pub = 0;
    unsigned phi = 0;
#pragma nounroll
    for (int t = 2; t < NT + 3; ++t) {
      if (t >= NSLOT && t < NT) {
        const unsigned needt = (unsigned)(t - NSLOT);
        WAIT(true, 1) {
          const v4u nd = *(volatile LAS v4u*)(L + C_OFF + 16);
          const unsigned mn = min(min(nd.x, nd.y), min(nd.z, nd.w));
          if (RFL(mn) > needt) break;
          SLEEP(1);
        }
        FENCE();
      }
      const v2u rf = *(volatile LAS v2u*)(L + C_OFF + 48);
      const unsigned rvalid = RFL(rf.x), ref = RFL(rf.y);
      const int e = t >= NT ? (t == NT ? TH2 + 1 : t == NT + 1 ? TH1 + 1 : 0) : rvalid ? (int)(phi - ref) : 0;
      const unsigned tt = ((unsigned)t * TAU) >> 4;
      const int issued = t < NT ? t : NT;
      if (t == NKT) src = vb;
      int landed; unsigned adj;
      if (e > TH2) { asm volatile("s_waitcnt vmcnt(47)" ::: "memory"); landed = issued - 3; adj = (31 * TAU) >> 8; }
      else if (e > TH1) { asm volatile("s_waitcnt vmcnt(32)" ::: "memory"); landed = issued - 2; adj = TAU >> 4; }
      else {
        asm volatile("s_waitcnt vmcnt(16)" ::: "memory"); landed = issued - 1; adj = 0;
        if (e < 0) { const unsigned due = ref + tt - LEAD; SPIN sp < 64 && (int)(now() - due) < 0; ++sp) SLEEP(1); }
      }
      if (t < NT) {
        const unsigned dst = ldsbase + (t & (NSLOT - 1)) * TILE_B;
        const unsigned ti = (unsigned)(t & (NKT - 1));
        const u64 s0 = src + ((ti * TILE_B) ^ (ti < 2 ? 0u : xm));
        const u64 s1 = s0 + 4096, s2 = s0 + 8192, s3 = s0 + 12288;
        unsigned keep;
        asm volatile("s_mov_b32 %0, m0\n\t"
                     "s_mov_b32 m0, %9\n\ts_nop 0\n\t" DMA4("%5")
                     "s_add_u32 m0, %9, 0x1000\n\ts_nop 0\n\t" DMA4("%6")
                     "s_add_u32 m0, %9, 0x2000\n\ts_nop 0\n\t" DMA4("%7")
                     "s_add_u32 m0, %9, 0x3000\n\ts_nop 0\n\t" DMA4("%8")
                     "s_mov_b32 m0, %0"
                     : "=&s"(keep) : "v"(o0), "v"(o1), "v"(o2), "v"(o3), "s"(s0), "s"(s1), "s"(s2), "s"(s3), "s"(dst) : "memory", "scc");
      }
      if (landed > pub) pub = landed;
      phi = now() - tt + adj;
      *(volatile LAS v2u*)(L + C_OFF) = v2u{(unsigned)pub, phi};
    }
    DRAIN();
    ctl[0] = (unsigned)NT;
    return;
  }
  const int s = bid & 3, kvh = (bid >> 2) & 7, b = bid >> 5;
  const bool has_out = (bid >> 5) < 7;
  const int lc = has_out ? (bid >> 5) : 0, rg = (bid & 7) * 4 + ((bid >> 3) & 3);
  auto wait_full = [&](unsigned n) { WAIT(RFL(ctl[0]) < n, 1) { SLEEP(1); } FENCE(); };
  auto wait_full_slow = [&](unsigned n) { WAIT(RFL(ctl[0]) < n, 4) { SLEEP(4); } FENCE(); };
  if (wave == 5) {
    WAIT(RFL(ctl[2]) == 0u, 4) { SLEEP(4); }
    const unsigned tag = RFL(ctl[3]);
    int mixs = 4;
#pragma nounroll
    for (int k = PK0; k + PKD < WO_AT; k += PKG) {
      wait_full_slow(k);
      const unsigned mine = RFL(ctl[1]);
      if (lane == 0) __hip_atomic_store((GAS u64*)(ws + WS_PH) + bid * 16, ((u64)tag << 32) | mine, RLX_AGENT);
      wait_full_slow(k + PKD);
      u64 o = 0;
      if (lane < 8) o = __hip_atomic_load((GAS u64*)(ws + WS_PH) + (((((bid >> 3) + mixs * lane) & 31) << 3) + lane) * 16, RLX_AGENT);
      mixs = 5 - mixs;
      const bool ok = (unsigned)(o >> 32) == tag && lane < 8;
      const float dsum = wavesum(ok ? (float)(int)((unsigned)o - mine) : 0.f);
      const int n = __builtin_popcountll(__builtin_amdgcn_ballot_w64(ok));
      if (n >= 4) { const unsigned ref = mine + (unsigned)(int)(dsum / (float)n); *(volatile LAS v2u*)(L + C_OFF + 48) = v2u{1u, ref}; }
    }
    if (!has_out) return;
    const unsigned char* wsrc = (const unsigned char*)wo + (size_t)(rg * 128 + (lane >> 3)) * (D * 2) + lc * 128 + (lane & 7) * 16;
    wait_full(WO_AT);
    WAIT(RFL(ctl[9]) < NCONS, 4) { SLEEP(4); }
    FENCE();
#pragma unroll
    for (int m = 0; m < 16; ++m) __builtin_amdgcn_global_load_lds((const GAS unsigned*)(wsrc + m * 8 * (D * 2)), (LAS unsigned*)(L + S_OFF + m * 1024), 16, 0, 2);
    DRAIN();
    ctl[11] = 1u;
    return;
  }
  const __amdgpu_buffer_rsrc_t rs = __builtin_amdgcn_make_buffer_rsrc((void*)ws, 0, WS_BYTES, 0x00020000);
  const int w = wave - 1;
  const int g = lane >> 4, r16 = lane & 15, head = lane & 1;
  const int qq = (lane >> 2) & 3, pp = lane & 3;
  unsigned tag, eraw = 0;
  int eoff = WS_BYTES;
  float oscale;
  {
    unsigned ka[4];
#pragma unroll
    for (int i = 0; i < 4; ++i) ka[i] = r16 * 512 + (((4 * i + g) ^ (2 * (r16 & 7))) << 4);
    v4u pre[NPRE][16];
    if (w == 0) {
      const unsigned tq0 = now();
      ctl[4] = (unsigned)(3 * NPRE);
      {
        const int r = (lane & 7) < 7 ? (lane & 7) : (lane >> 3), word = (lane & 7) < 7 ? (lane >> 3) : 8;
        const int end = r == 0 ? 128 : r == 1 ? WS_QG : r == 2 ? WS_ST : r == 3 ? WS_PO : r == 4 ? WS_Y2 : r == 5 ? WS_PH : WS_BYTES;
        eoff = r == 7 ? WS_BYTES : end - 128 + word * 4;
      }
      eraw = __builtin_amdgcn_raw_buffer_load_b32(rs, eoff, 0, 16);
      const int ctile = (bid & 7) * 32 + (bid >> 3);
      {
        const unsigned short* ps = wq + (size_t)((bid >> 3) * 14) * 4096 + (bid & 7) * 512 + lane * 8;
#pragma unroll
        for (int j = 0; j < 14; ++j) __builtin_amdgcn_global_load_lds((const GAS unsigned*)(ps + (size_t)j * 4096), (LAS unsigned*)(L + S_OFF + j * 1024), 16, 0, 0);
        DRAIN();
        SLEEP(PFD);
      }
      const unsigned short* wsrc = wq + (size_t)(lane >> 1) * 4096 + ctile * 16 + (lane & 1) * 8;
#pragma unroll
      for (int j = 0; j < 14; ++j) __builtin_amdgcn_global_load_lds((const GAS unsigned*)(wsrc + (size_t)j * 32 * 4096), (LAS unsigned*)(L + S_OFF + j * 1024), 16, 0, 2);
#pragma unroll
      for (int j = 0; j < 7; ++j) __builtin_amdgcn_global_load_lds((const GAS unsigned*)(x + j * 512 + lane * 8), (LAS unsigned*)(L + X_OFF + j * 1024), 16, 0, 0);
      __builtin_amdgcn_sched_barrier(0);
      DRAIN();
      asm volatile("" : "+v"(eraw));
      {
        unsigned e = eraw;
#pragma unroll
        for (int m = 32; m >= 1; m >>= 1) e += (unsigned)__shfl_xor((int)e, m);
        tag = RFL(e) + 1u;
      }
      *(volatile LAS v2u*)(L + C_OFF + 8) = v2u{1u, tag};
      v4u xf[14], wf[14];
      LAS unsigned char* wb = L + S_OFF + (8 * g + qq) * 32 + pp * 8;
#pragma unroll
      for (int j = 0; j < 14; ++j) {
        xf[j] = *(const LAS v4u*)(L + X_OFF + (lane & 7) * (D * 2) + 64 * j + 16 * g);
        const v2u lo = trread(wb + j * 1024), hi = trread(wb + j * 1024 + 128); wf[j] = v4u{lo.x, lo.y, hi.x, hi.y};
      }
      __builtin_amdgcn_sched_barrier(0);
      f32x4 d = {0.f, 0.f, 0.f, 0.f}, d2 = {0.f, 0.f, 0.f, 0.f};
#pragma unroll
      for (int j = 0; j < 14; j += 2) { d = mfma(wf[j], xf[j], d); d2 = mfma(wf[j + 1], xf[j + 1], d2); }
      d += d2;
      ST_SC1((v4u{cvtpk(d[0], d[1]), tag, cvtpk(d[2], d[3]), tag}), r16 < 8 ? WS_QG + (r16 * 2048 + ctile * 8 + 2 * g) * 8 : WS_BYTES);
      const int qo = WS_QG + (b * 2048 + kvh * 256) * 8 + lane * 16;
      v4u ga = {0u, 0u, 0u, 0u}, gb = ga;
      SPIN sp < 1024 && (int)(now() - tq0) < QD; ++sp) SLEEP(2);
      WAIT(true, 16) {
        ga = LD_SC1(qo);
        gb = LD_SC1(qo + 1024);
        if (__all(ga.y == tag && ga.w == tag && gb.y == tag && gb.w == tag)) break;
        SLEEP(8);
        FENCE();
      }
      *(LAS v2u*)(L + Q_OFF + lane * 8) = v2u{ga.x, ga.z};
      *(LAS v2u*)(L + Q_OFF + 512 + lane * 8) = v2u{gb.x, gb.z};
      LDSW();
      ctl[10] = 1u;
    } else {
#pragma unroll
      for (int j = 0; j < NPRE; ++j) {
        const int t = (w - 1) + 3 * j;
        wait_full(t + 1);
        LAS unsigned char* tb = L + (t & (NSLOT - 1)) * TILE_B;
#pragma unroll
        for (int i = 0; i < 8; ++i) { pre[j][i] = *(const LAS v4u*)(tb + ka[i & 3] + (i >> 2) * 256); pre[j][8 + i] = *(const LAS v4u*)(tb + ka[i & 3] + (i >> 2) * 256 + 8192); }
        LDSW();
        ctl[4 + w] = (unsigned)(j + 1 < NPRE ? t + 3 : 3 * NPRE + w);
        PIN16(pre[j]);
      }
      WAIT(RFL(ctl[10]) == 0u, 2) { SLEEP(2); }
      FENCE();
      tag = RFL(ctl[3]);
    }
    v4u qf[8];
#pragma unroll
    for (int i = 0; i < 8; ++i) qf[i] = *(const LAS v4u*)(L + Q_OFF + head * 512 + 64 * i + 16 * g);
    float mx = -INFINITY;
    auto qk = [&](const v4u (&a)[16], int t) {
      f32x4 d0 = {0.f, 0.f, 0.f, 0.f}, d1 = {0.f, 0.f, 0.f, 0.f};
#pragma unroll
      for (int i = 0; i < 8; ++i) { d0 = mfma(a[i], qf[i], d0); d1 = mfma(a[8 + i], qf[i], d1); }
      mx = fmaxf(mx, fmaxf(fmaxf(fmaxf(d0[0], d0[1]), fmaxf(d0[2], d0[3])), fmaxf(fmaxf(d1[0], d1[1]), fmaxf(d1[2], d1[3]))));
      if (r16 < 4) {
        const f32x4 d = (r16 & 2) ? d1 : d0;
        *(LAS f32x4*)(L + S_OFF + head * (KPS * 4) + (t * 32 + (r16 & 2) * 8 + 4 * g) * 4) = d;
      }
    };
    if (w > 0) {
#pragma unroll
      for (int j = 0; j < NPRE; ++j) qk(pre[j], (w - 1) + 3 * j);
    }
#pragma nounroll
    for (int t = 3 * NPRE + w; t < NKT; t += NCONS) {
      wait_full(t + 1);
      LAS unsigned char* tb = L + (t & (NSLOT - 1)) * TILE_B;
      v4u a[16];
#pragma unroll
      for (int i = 0; i < 8; ++i) { a[i] = *(const LAS v4u*)(tb + ka[i & 3] + (i >> 2) * 256); a[8 + i] = *(const LAS v4u*)(tb + ka[i & 3] + (i >> 2) * 256 + 8192); }
      LDSW();
      ctl[4 + w] = (unsigned)(t + NCONS < NKT ? t + NCONS : NKT);
      PIN16(a);
      qk(a, t);
    }
    mx = x16max(mx);
    if (lane < 2) ((LAS float*)(L + C_OFF))[16 + w * 2 + lane] = mx;
    LDSW();
    if (lane == 0) __hip_atomic_fetch_add((LAS unsigned*)(L + C_OFF) + 8, 1u, RLX_WG);
    WAIT(RFL(ctl[8]) < NCONS, 1) { SLEEP(1); }
    FENCE();
    constexpr float C2 = 0.0625f * 1.4426950408889634f;
    const f32x4 ma = *(const LAS f32x4*)(L + C_OFF + 64), mb = *(const LAS f32x4*)(L + C_OFF + 80);
    const float M0 = fmaxf(fmaxf(ma[0], ma[2]), fmaxf(mb[0], mb[2])) * C2, M1 = fmaxf(fmaxf(ma[1], ma[3]), fmaxf(mb[1], mb[3])) * C2;
    const int tt = w * 64 + lane, tile = tt >> 2, gg = tt & 3;
    float sum[2];
#pragma unroll
    for (int h = 0; h < 2; ++h) {
      const f32x4 sa = *(const LAS f32x4*)(L + S_OFF + h * (KPS * 4) + (tile * 32 + 4 * gg) * 4);
      const f32x4 sb = *(const LAS f32x4*)(L + S_OFF + h * (KPS * 4) + (tile * 32 + 16 + 4 * gg) * 4);
      const float mc = h ? M1 : M0;
      float p[8];
#pragma unroll
      for (int j = 0; j < 4; ++j) { p[j] = __builtin_amdgcn_exp2f(fmaf(sa[j], C2, -mc)); p[4 + j] = __builtin_amdgcn_exp2f(fmaf(sb[j], C2, -mc)); }
      sum[h] = ((p[0] + p[1]) + (p[2] + p[3])) + ((p[4] + p[5]) + (p[6] + p[7]));
      *(LAS v4u*)(L + P_OFF + tile * 128 + gg * 32 + h * 16) = v4u{cvtpk(p[0], p[1]), cvtpk(p[2], p[3]), cvtpk(p[4], p[5]), cvtpk(p[6], p[7])};
    }
    sum[0] = wavesum(sum[0]); sum[1] = wavesum(sum[1]);
    if (lane < 2) ((LAS float*)(L + C_OFF))[24 + w * 2 + lane] = lane ? sum[1] : sum[0];
    LDSW();
    if (lane == 0) __hip_atomic_fetch_add((LAS unsigned*)(L + C_OFF) + 9, 1u, RLX_WG);
    WAIT(RFL(ctl[9]) < NCONS, 1) { SLEEP(1); }
    FENCE();
    const f32x4 la = *(const LAS f32x4*)(L + C_OFF + 96), lb = *(const LAS f32x4*)(L + C_OFF + 112);
    const float Ls = head ? ((la[1] + la[3]) + (lb[1] + lb[3])) : ((la[0] + la[2]) + (lb[0] + lb[2]));
    oscale = 1.0f / Ls;
    if (w == 1) ST_SC1((v4u{__float_as_uint(head ? M1 : M0), tag, __float_as_uint(Ls), tag}), lane < 2 ? WS_ST + (((kvh * 2 + head) * 8 + b) * 4 + s) * 16 : WS_BYTES);
  }
  const int u = (r16 >> 1) & 3;
  int ooff = r16 < 8 ? WS_PO + ((b * 16 + kvh * 2 + head) * 4 + s) * 1024 + (64 * w + 16 * u + 4 * g) * 4 : WS_BYTES;
  asm volatile("" : "+v"(ooff), "+v"(oscale));
  f32x4 acc[4];
#pragma unroll
  for (int k = 0; k < 4; ++k) acc[k] = f32x4{0.f, 0.f, 0.f, 0.f};
  unsigned tgate = 0;
  {
    const int key = 4 * g + qq;
    unsigned va[4];
#pragma unroll
    for (int k = 0; k < 4; ++k) va[k] = key * 512 + (((8 * w + 2 * k + (pp >> 1)) ^ (2 * (key & 7))) << 4) + (pp & 1) * 8;
#pragma nounroll
    for (int t = 0; t < NKT; ++t) {
      v4u pf = *(const LAS v4u*)(L + P_OFF + t * 128 + g * 32 + head * 16);
      if (t == NKT - 1) tgate = RFL(ctl[13]) + ((NT * TAU) >> 4) + G1;
      wait_full(NKT + t + 1);
      LAS unsigned char* tb = L + (t & (NSLOT - 1)) * TILE_B;
      v4u av[4];
#pragma unroll
      for (int k = 0; k < 4; ++k) { const v2u lo = trread(tb + va[k]), hi = trread(tb + va[k] + 8192); av[k] = v4u{lo.x, lo.y, hi.x, hi.y}; }
      LDSW();
      ctl[4 + w] = (unsigned)(NKT + t + 1);
      asm volatile("" : "+v"(av[0]), "+v"(av[1]), "+v"(av[2]), "+v"(av[3]), "+v"(pf) :: "memory");
#pragma unroll
      for (int k = 0; k < 4; ++k) acc[k] = mfma(av[k], pf, acc[k]);
    }
  }
  {
    const f32x4 a01 = (u & 1) ? acc[1] : acc[0], a23 = (u & 1) ? acc[3] : acc[2];
    const f32x4 a = ((u & 2) ? a23 : a01) * oscale;
    ST_SC1((v4u{cvtpk(a[0], a[1]), tag, cvtpk(a[2], a[3]), tag}), ooff);
  }
  if (!has_out) return;
  const u64 wsa = (u64)ws;
  const v4i srd = {(int)(unsigned)wsa, (int)((unsigned)(wsa >> 32) & 0xffffu), WS_BYTES, 0x00020000};
  v4u p0, p1, p2, p3;
  auto gather_issue = [&](int voff) {
    asm volatile("buffer_load_dwordx4 %0, %4, %5, 0 offen sc1\n\t"
                 "buffer_load_dwordx4 %1, %4, %5, 0 offen offset:1024 sc1\n\t"
                 "buffer_load_dwordx4 %2, %4, %5, 0 offen offset:2048 sc1\n\t"
                 "buffer_load_dwordx4 %3, %4, %5, 0 offen offset:3072 sc1"
                 : "=&v"(p0), "=&v"(p1), "=&v"(p2), "=&v"(p3) : "v"(voff), "s"(srd) : "memory");
  };
  auto gather_wait = [&](int voff) {
    int sp = 0;
    WAIT(true, 16) {
      asm volatile("s_waitcnt vmcnt(0)" : "+v"(p0), "+v"(p1), "+v"(p2), "+v"(p3) :: "memory");
      const u64 m0 = __builtin_amdgcn_ballot_w64(p0.y != tag || p0.w != tag), m1 = __builtin_amdgcn_ballot_w64(p1.y != tag || p1.w != tag);
      const u64 m2 = __builtin_amdgcn_ballot_w64(p2.y != tag || p2.w != tag), m3 = __builtin_amdgcn_ballot_w64(p3.y != tag || p3.w != tag);
      if (!(m0 | m1 | m2 | m3)) break;
      if (++sp > 16) SLEEP(8);
      asm volatile("s_mov_b64 exec, %5\n\tbuffer_load_dwordx4 %0, %4, %9, 0 offen sc1\n\t"
                   "s_mov_b64 exec, %6\n\tbuffer_load_dwordx4 %1, %4, %9, 0 offen offset:1024 sc1\n\t"
                   "s_mov_b64 exec, %7\n\tbuffer_load_dwordx4 %2, %4, %9, 0 offen offset:2048 sc1\n\t"
                   "s_mov_b64 exec, %8\n\tbuffer_load_dwordx4 %3, %4, %9, 0 offen offset:3072 sc1\n\t"
                   "s_mov_b64 exec, -1"
                   : "+v"(p0), "+v"(p1), "+v"(p2), "+v"(p3) : "v"(voff), "s"(m0), "s"(m1), "s"(m2), "s"(m3), "s"(srd) : "memory");
    }
    asm volatile("s_waitcnt vmcnt(0)" : "+v"(p0), "+v"(p1), "+v"(p2), "+v"(p3) :: "memory");
  };
  {
    int po = WS_PO + ((lane >> 3) * 16 + (rg >> 1)) * 4096 + ((rg & 1) * 128 + 32 * w) * 4 + (lane & 7) * 16;
    const int so = WS_ST + (rg >> 1) * 512 + (lane & 31) * 16;
    v4u sc;
    asm volatile("buffer_load_dwordx4 %0, %2, %3, 0 offen sc1" : "=&v"(sc), "+v"(po) : "v"(so), "s"(srd) : "memory");
    SPIN sp < 32 && (int)(now() - tgate) < 0; ++sp) SLEEP(1);
    gather_issue(po);
    asm volatile("s_waitcnt vmcnt(4)" : "+v"(sc) :: "memory");
    WAIT(!__all(sc.y == tag && sc.w == tag), 16) {
      SLEEP(8);
      asm volatile("buffer_load_dwordx4 %0, %1, %2, 0 offen sc1\n\ts_waitcnt vmcnt(0)" : "=&v"(sc) : "v"(so), "s"(srd) : "memory");
    }
    f32x4 wya, wyb;
    {
      const float ms = __uint_as_float(sc.x), ls = __uint_as_float(sc.z);
      float M = fmaxf(ms, dpp<XOR1>(ms)); M = fmaxf(M, dpp<XOR2>(M));
      const float e = __builtin_amdgcn_exp2f(ms - M) * ls;
      float Lt = e + dpp<XOR1>(e); Lt += dpp<XOR2>(Lt);
      LAS unsigned char* qb = L + Q_OFF + w * 128;
      *(LAS float*)(qb + (lane & 3) * 32 + ((lane >> 2) & 7) * 4) = e / Lt;
      LDSW();
      wya = *(const LAS f32x4*)(qb + g * 16); wyb = *(const LAS f32x4*)(qb + 64 + g * 16);
    }
    WAIT(RFL(ctl[11]) == 0u, 1) { SLEEP(1); }
    FENCE();
    v4u wa[4];
    {
      LAS unsigned char* wb = L + S_OFF + (8 * g + qq) * 128 + w * 32 + pp * 8;
#pragma unroll
      for (int k = 0; k < 4; ++k) { const v2u lo = trread(wb + k * 4096), hi = trread(wb + k * 4096 + 512); wa[k] = v4u{lo.x, lo.y, hi.x, hi.y}; }
    }
    const int yo = WS_Y2 + ((((g & 1) * 4 + (g >> 1)) * 7 + lc) * 4 + w) * 4096 + rg * 128 + r16 * 8;
    gather_wait(po);
    LAS unsigned char* xb = L + X_OFF + w * 2048 + lane * 8;
    *(LAS v2u*)(xb) = v2u{p0.x, p0.z}; *(LAS v2u*)(xb + 512) = v2u{p1.x, p1.z};
    *(LAS v2u*)(xb + 1024) = v2u{p2.x, p2.z}; *(LAS v2u*)(xb + 1536) = v2u{p3.x, p3.z};
    LDSW();
    __builtin_amdgcn_s_barrier();
    FENCE();
    v4u oa[8];
#pragma unroll
    for (int k = 0; k < 8; ++k) oa[k] = *(const LAS v4u*)(L + X_OFF + k * 1024 + r16 * 64 + g * 16);
    f32x4 ya = {0.f, 0.f, 0.f, 0.f}, yb = {0.f, 0.f, 0.f, 0.f};
#pragma unroll
    for (int k = 0; k < 4; ++k) { ya = mfma(oa[2 * k], wa[k], ya); yb = mfma(oa[2 * k + 1], wa[k], yb); }
    ya = ya * wya + yb * wyb;
    auto t0 = __builtin_amdgcn_permlane32_swap(__float_as_uint(ya[0]), __float_as_uint(ya[1]), false, false);
    auto t1 = __builtin_amdgcn_permlane32_swap(__float_as_uint(ya[2]), __float_as_uint(ya[3]), false, false);
    const float v0 = __uint_as_float(t0[0]) + __uint_as_float(t0[1]), v1 = __uint_as_float(t1[0]) + __uint_as_float(t1[1]);
    __builtin_amdgcn_raw_buffer_store_b64(v2u{__float_as_uint(v0), tag}, rs, yo, 0, 16);
    __builtin_amdgcn_raw_buffer_store_b64(v2u{__float_as_uint(v1), tag}, rs, yo + 2 * 7 * 4 * 4096, 0, 16);
  }
  if (w != 0) return;
  {
    const int nt = (bid >> 3) & 3, b3 = bid & 7;
    const int yo = WS_Y2 + ((b3 * 7 + lc) * 4 + nt) * 4096 + lane * 16;
    SLEEP(D2);
    gather_issue(yo);
    gather_wait(yo);
    float v0 = (__uint_as_float(p0.x) + __uint_as_float(p1.x)) + (__uint_as_float(p2.x) + __uint_as_float(p3.x));
    float v1 = (__uint_as_float(p0.z) + __uint_as_float(p1.z)) + (__uint_as_float(p2.z) + __uint_as_float(p3.z));
    v0 += dpp<XOR8>(v0); v1 += dpp<XOR8>(v1);
    v0 = x16sum(v0); v1 = x16sum(v1);
    const __amdgpu_buffer_rsrc_t ro = __builtin_amdgcn_make_buffer_rsrc((void*)out, 0, B * D * 2, 0x00020000);
    __builtin_amdgcn_raw_buffer_store_b32(cvtpk(v0, v1), ro, lane < 8 ? (b3 * D + lc * 64 + nt * 16 + 2 * lane) * 2 : B * D * 2, 0, 0);
    __builtin_amdgcn_raw_buffer_store_b32(eraw + 1u, rs, bid == 0 ? eoff : WS_BYTES, 0, 16);
  }
}

extern "C" void kernel_launch(void* const* d_in, const int* in_sizes, int n_in, void* d_out, int out_size, void* d_ws, size_t ws_size, hipStream_t stream) {
  (void)hipFuncSetAttribute((const void*)k_fused, hipFuncAttributeMaxDynamicSharedMemorySize, LDS_BYTES);
  k_fused<<<256, NWAVE * 64, LDS_BYTES, stream>>>((const unsigned short*)d_in[0], (const unsigned short*)d_in[1], (const unsigned short*)d_in[2],
      (const unsigned short*)d_in[3], (const unsigned short*)d_in[4], (unsigned short*)d_out, (unsigned char*)d_ws);
}
```

```cpp
#include <hip/hip_runtime.h>
#include <hip/hip_bf16.h>
#include <stdint.h>

typedef unsigned v4u __attribute__((ext_vector_type(4)));
typedef unsigned v2u __attribute__((ext_vector_type(2)));
typedef int v4i __attribute__((ext_vector_type(4)));
typedef float f32x4 __attribute__((ext_vector_type(4)));
typedef float f32x2 __attribute__((ext_vector_type(2)));
typedef short s16x4 __attribute__((ext_vector_type(4)));
typedef __bf16 bf16x8 __attribute__((ext_vector_type(8)));
typedef __bf16 bf16x2 __attribute__((ext_vector_type(2)));
typedef unsigned long long u64;
#define LAS __attribute__((address_space(3)))
#define GAS __attribute__((address_space(1)))
#define RLX_AGENT __ATOMIC_RELAXED, __HIP_MEMORY_SCOPE_AGENT
#define RLX_WG __ATOMIC_RELAXED, __HIP_MEMORY_SCOPE_WORKGROUP

constexpr int B = 8, D = 448, DH = 256;
constexpr int KPS = 2048;
#define TAU 880
#define LEAD 30
#define TH1 40
#define TH2 150
#define PK0 8
#define PKG 24
#define PKD 3
#define PFD 32
#define QD 550
#define WO_AT 127
#define G1 100
#define D2 15
constexpr int NPRE = 3;
constexpr int TILE_B = 16384, NSLOT = 8, NKT = KPS / 32, NT = 2 * NKT;
constexpr int NCONS = 4, NWAVE = 6;
constexpr int WAIT_BUDGET = 1 << 22;
constexpr int S_OFF = NSLOT * TILE_B;
constexpr int X_OFF = S_OFF + 2 * KPS * 4;
constexpr int P_OFF = X_OFF + 2048;
constexpr int Q_OFF = P_OFF + NKT * 128;
constexpr int C_OFF = Q_OFF + 1024;
constexpr int LDS_BYTES = C_OFF + 256;
constexpr int WS_QG = 4096;
constexpr int WS_ST = WS_QG + 8 * 2048 * 8 + 1024;
constexpr int WS_PO = WS_ST + 16 * 8 * 4 * 16 + 1024;
constexpr int WS_Y2 = WS_PO + 8 * 16 * 4 * 1024 + 1024;
constexpr int WS_PH = WS_Y2 + 8 * 7 * 4 * 4096 + 1024;
constexpr int WS_BYTES = WS_PH + 256 * 128 + 1024;

__device__ __forceinline__ unsigned cvtpk(float lo, float hi) { f32x2 v = {lo, hi}; return __builtin_bit_cast(unsigned, __builtin_convertvector(v, bf16x2)); }
__device__ __forceinline__ f32x4 mfma(v4u a, v4u b, f32x4 c) {
  return __builtin_amdgcn_mfma_f32_16x16x32_bf16(__builtin_bit_cast(bf16x8, a), __builtin_bit_cast(bf16x8, b), c, 0, 0, 0);
}
__device__ __forceinline__ v2u trread(LAS unsigned char* p) { return __builtin_bit_cast(v2u, __builtin_amdgcn_ds_read_tr16_b64_v4i16((LAS s16x4*)p)); }
__device__ __forceinline__ float x16max(float x) {
  auto s = __builtin_amdgcn_permlane16_swap(__float_as_uint(x), __float_as_uint(x), false, false);
  x = fmaxf(__uint_as_float(s[0]), __uint_as_float(s[1]));
  auto t = __builtin_amdgcn_permlane32_swap(__float_as_uint(x), __float_as_uint(x), false, false);
  return fmaxf(__uint_as_float(t[0]), __uint_as_float(t[1]));
}
__device__ __forceinline__ float x16sum(float x) {
  auto s = __builtin_amdgcn_permlane16_swap(__float_as_uint(x), __float_as_uint(x), false, false);
  x = __uint_as_float(s[0]) + __uint_as_float(s[1]);
  auto t = __builtin_amdgcn_permlane32_swap(__float_as_uint(x), __float_as_uint(x), false, false);
  return __uint_as_float(t[0]) + __uint_as_float(t[1]);
}
template <int CTRL> __device__ __forceinline__ float dpp(float x) {
  return __builtin_bit_cast(float, __builtin_amdgcn_mov_dpp(__builtin_bit_cast(int, x), CTRL, 0xf, 0xf, true));
}
constexpr int XOR1 = 0xB1, XOR2 = 0x4E, XOR7 = 0x141, XOR8 = 0x128;
__device__ __forceinline__ float wavesum(float x) {
  x += dpp<XOR1>(x); x += dpp<XOR2>(x); x += dpp<XOR7>(x); x += dpp<XOR8>(x);
  return x16sum(x);
}
__device__ __forceinline__ unsigned now() { return (unsigned)__builtin_amdgcn_s_memrealtime(); }
#define LDSW() asm volatile("s_waitcnt lgkmcnt(0)" ::: "memory")
#define DRAIN() asm volatile("s_waitcnt vmcnt(0)" ::: "memory")
#define FENCE() asm volatile("" ::: "memory")
#define PIN16(a) asm volatile("" : "+v"(a[0]), "+v"(a[1]), "+v"(a[2]), "+v"(a[3]), "+v"(a[4]), "+v"(a[5]), "+v"(a[6]), "+v"(a[7]), \
                        "+v"(a[8]), "+v"(a[9]), "+v"(a[10]), "+v"(a[11]), "+v"(a[12]), "+v"(a[13]), "+v"(a[14]), "+v"(a[15]) :: "memory")
#define RFL(x) __builtin_amdgcn_readfirstlane(x)
#define SLEEP(n) __builtin_amdgcn_s_sleep(n)
#define LD_SC1(off) __builtin_amdgcn_raw_buffer_load_b128(rs, (off), 0, 16)
#define ST_SC1(v, off) __builtin_amdgcn_raw_buffer_store_b128((v), rs, (off), 0, 16)
#define SPIN _Pragma("nounroll") for (int sp = 0;
#define WAIT(cond, cost) _Pragma("nounroll") for (; bud > 0 && (cond); bud -= (cost))

__global__ void __launch_bounds__(NWAVE * 64, 2) k_fused(const unsigned short* __restrict__ x, const unsigned short* __restrict__ kc, const unsigned short* __restrict__ vc,
                                              const unsigned short* __restrict__ wq, const unsigned short* __restrict__ wo, unsigned short* __restrict__ out, unsigned char* ws) {
  extern __shared__ __attribute__((aligned(16))) unsigned char lds[];
  LAS unsigned char* L = (LAS unsigned char*)lds;
  volatile LAS unsigned* ctl = (volatile LAS unsigned*)(L + C_OFF);
  const int bid = blockIdx.x;
  const int lane = threadIdx.x & 63, wave = RFL(threadIdx.x >> 6);
  int bud = WAIT_BUDGET;
  const unsigned o0 = (lane >> 5) * 512 + (((lane & 31) ^ ((lane >> 5) << 1)) << 4);
  const unsigned o1 = o0 ^ 64, o2 = o0 ^ 128, o3 = o0 ^ 192;
  const unsigned ldsbase = (unsigned)(size_t)L;
  u64 src = (u64)kc + (u64)bid * (KPS * DH * 2);
#define DMA4(S) "global_load_lds_dwordx4 %1, " S " nt\n\tglobal_load_lds_dwordx4 %2, " S " offset:1024 nt\n\t" \
                "global_load_lds_dwordx4 %3, " S " offset:2048 nt\n\tglobal_load_lds_dwordx4 %4, " S " offset:3072 nt\n\t"
  {
    unsigned keep, cnt, c0 = o0, c1 = o1, c2 = o2, c3 = o3;
    asm volatile("s_cmp_lg_u32 %8, 0\n\ts_cbranch_scc1 .Lskip%=\n\t"
                 "s_setprio 3\n\t"
                 "s_mov_b32 %0, m0\n\ts_mov_b32 m0, %7\n\ts_mov_b32 %5, 8\n"
                 ".Lloop%=:\n\t" DMA4("%6")
                 "s_add_u32 m0, m0, 0x1000\n\t"
                 "v_add_u32 %1, 0x1000, %1\n\tv_add_u32 %2, 0x1000, %2\n\tv_add_u32 %3, 0x1000, %3\n\tv_add_u32 %4, 0x1000, %4\n\t"
                 "s_sub_u32 %5, %5, 1\n\ts_cmp_lg_u32 %5, 0\n\ts_cbranch_scc1 .Lloop%=\n\t"
                 "s_mov_b32 m0, %0\n"
                 ".Lskip%=:"
                 : "=&s"(keep), "+v"(c0), "+v"(c1), "+v"(c2), "+v"(c3), "=&s"(cnt)
                 : "s"(src), "s"(ldsbase), "s"(wave) : "memory", "scc");
  }
  if (threadIdx.x < 64) ((LAS unsigned*)(L + C_OFF))[lane] = 0u;
  LDSW();
  __builtin_amdgcn_s_barrier();
  if (wave == 0) {
    const u64 vb = (u64)vc + (u64)bid * (KPS * DH * 2);
    const unsigned xm = (unsigned)(bid & 1) * TILE_B;
    int pub = 0;
    unsigned phi = 0;
#pragma nounroll
    for (int t = 2; t < NT + 3; ++t) {
      if (t >= NSLOT && t < NT) {
        const unsigned needt = (unsigned)(t - NSLOT);
        WAIT(true, 1) {
          const v4u nd = *(volatile LAS v4u*)(L + C_OFF + 16);
          const unsigned mn = min(min(nd.x, nd.y), min(nd.z, nd.w));
          if (RFL(mn) > needt) break;
          SLEEP(1);
        }
        FENCE();
      }
      const v2u rf = *(volatile LAS v2u*)(L + C_OFF + 48);
      const unsigned rvalid = RFL(rf.x), ref = RFL(rf.y);
      const int e = t >= NT ? (t == NT ? TH2 + 1 : t == NT + 1 ? TH1 + 1 : 0) : rvalid ? (int)(phi - ref) : 0;
      const unsigned tt = ((unsigned)t * TAU) >> 4;
      const int issued = t < NT ? t : NT;
      if (t == NKT) src = vb;
      int landed; unsigned adj;
      if (e > TH2) { asm volatile("s_waitcnt vmcnt(47)" ::: "memory"); landed = issued - 3; adj = (31 * TAU) >> 8; }
      else if (e > TH1) { asm volatile("s_waitcnt vmcnt(32)" ::: "memory"); landed = issued - 2; adj = TAU >> 4; }
      else {
        asm volatile("s_waitcnt vmcnt(16)" ::: "memory"); landed = issued - 1; adj = 0;
        if (e < 0) { const unsigned due = ref + tt - LEAD; SPIN sp < 64 && (int)(now() - due) < 0; ++sp) SLEEP(1); }
      }
      if (t < NT) {
        const unsigned dst = ldsbase + (t & (NSLOT - 1)) * TILE_B;
        const unsigned ti = (unsigned)(t & (NKT - 1));
        const u64 s0 = src + ((ti * TILE_B) ^ (ti < 2 ? 0u : xm));
        const u64 s1 = s0 + 4096, s2 = s0 + 8192, s3 = s0 + 12288;
        unsigned keep;
        asm volatile("s_mov_b32 %0, m0\n\t"
                     "s_mov_b32 m0, %9\n\ts_nop 0\n\t" DMA4("%5")
                     "s_add_u32 m0, %9, 0x1000\n\ts_nop 0\n\t" DMA4("%6")
                     "s_add_u32 m0, %9, 0x2000\n\ts_nop 0\n\t" DMA4("%7")
                     "s_add_u32 m0, %9, 0x3000\n\ts_nop 0\n\t" DMA4("%8")
                     "s_mov_b32 m0, %0"
                     : "=&s"(keep) : "v"(o0), "v"(o1), "v"(o2), "v"(o3), "s"(s0), "s"(s1), "s"(s2), "s"(s3), "s"(dst) : "memory", "scc");
      }
      if (landed > pub) pub = landed;
      phi = now() - tt + adj;
      *(volatile LAS v2u*)(L + C_OFF) = v2u{(unsigned)pub, phi};
    }
    DRAIN();
    ctl[0] = (unsigned)NT;
    return;
  }
  const int s = bid & 3, kvh = (bid >> 2) & 7, b = bid >> 5;
  const bool has_out = (bid >> 5) < 7;
  const int lc = has_out ? (bid >> 5) : 0, rg = (bid & 7) * 4 + ((bid >> 3) & 3);
  auto wait_full = [&](unsigned n) { WAIT(RFL(ctl[0]) < n, 1) { SLEEP(1); } FENCE(); };
  auto wait_full_slow = [&](unsigned n) { WAIT(RFL(ctl[0]) < n, 4) { SLEEP(4); } FENCE(); };
  if (wave == 5) {
    WAIT(RFL(ctl[2]) == 0u, 4) { SLEEP(4); }
    const unsigned tag = RFL(ctl[3]);
    int mixs = 4;
#pragma nounroll
    for (int k = PK0; k + PKD < WO_AT; k += PKG) {
      wait_full_slow(k);
      const unsigned mine = RFL(ctl[1]);
      if (lane == 0) __hip_atomic_store((GAS u64*)(ws + WS_PH) + bid * 16, ((u64)tag << 32) | mine, RLX_AGENT);
      wait_full_slow(k + PKD);
      u64 o = 0;
      if (lane < 8) o = __hip_atomic_load((GAS u64*)(ws + WS_PH) + (((((bid >> 3) + mixs * lane) & 31) << 3) + lane) * 16, RLX_AGENT);
      mixs = 5 - mixs;
      const bool ok = (unsigned)(o >> 32) == tag && lane < 8;
      const float dsum = wavesum(ok ? (float)(int)((unsigned)o - mine) : 0.f);
      const int n = __builtin_popcountll(__builtin_amdgcn_ballot_w64(ok));
      if (n >= 4) { const unsigned ref = mine + (unsigned)(int)(dsum / (float)n); *(volatile LAS v2u*)(L + C_OFF + 48) = v2u{1u, ref}; }
    }
    if (!has_out) return;
    const unsigned char* wsrc = (const unsigned char*)wo + (size_t)(rg * 128 + (lane >> 3)) * (D * 2) + lc * 128 + (lane & 7) * 16;
    wait_full(WO_AT);
    WAIT(RFL(ctl[9]) < NCONS, 4) { SLEEP(4); }
    FENCE();
#pragma unroll
    for (int m = 0; m < 16; ++m) __builtin_amdgcn_global_load_lds((const GAS unsigned*)(wsrc + m * 8 * (D * 2)), (LAS unsigned*)(L + S_OFF + m * 1024), 16, 0, 2);
    DRAIN();
    ctl[11] = 1u;
    return;
  }
  const __amdgpu_buffer_rsrc_t rs = __builtin_amdgcn_make_buffer_rsrc((void*)ws, 0, WS_BYTES, 0x00020000);
  const int w = wave - 1;
  const int g = lane >> 4, r16 = lane & 15, head = lane & 1;
  const int qq = (lane >> 2) & 3, pp = lane & 3;
  unsigned tag, eraw = 0;
  int eoff = WS_BYTES;
  float oscale;
  {
    unsigned ka[4];
#pragma unroll
    for (int i = 0; i < 4; ++i) ka[i] = r16 * 512 + (((4 * i + g) ^ (2 * (r16 & 7))) << 4);
    v4u pre[NPRE][16];
    if (w == 0) {
      const unsigned tq0 = now();
      ctl[4] = (unsigned)(3 * NPRE);
      {
        const int r = (lane & 7) < 7 ? (lane & 7) : (lane >> 3), word = (lane & 7) < 7 ? (lane >> 3) : 8;
        const int end = r == 0 ? 128 : r == 1 ? WS_QG : r == 2 ? WS_ST : r == 3 ? WS_PO : r == 4 ? WS_Y2 : r == 5 ? WS_PH : WS_BYTES;
        eoff = r == 7 ? WS_BYTES : end - 128 + word * 4;
      }
      const int ctile = (bid & 7) * 32 + (bid >> 3);
      {
        const unsigned short* ps = wq + (size_t)((bid >> 3) * 14) * 4096 + (bid & 7) * 512 + lane * 8;
#pragma unroll
        for (int j = 0; j < 14; ++j) __builtin_amdgcn_global_load_lds((const GAS unsigned*)(ps + (size_t)j * 4096), (LAS unsigned*)(L + S_OFF + j * 1024), 16, 0, 0);
        DRAIN();
        eraw = __builtin_amdgcn_raw_buffer_load_b32(rs, eoff, 0, 16);
        __builtin_amdgcn_sched_barrier(0);
        SLEEP(PFD);
      }
      const unsigned short* wsrc = wq + (size_t)(lane >> 1) * 4096 + ctile * 16 + (lane & 1) * 8;
#pragma unroll
      for (int j = 0; j < 14; ++j) __builtin_amdgcn_global_load_lds((const GAS unsigned*)(wsrc + (size_t)j * 32 * 4096), (LAS unsigned*)(L + S_OFF + j * 1024), 16, 0, 2);
#pragma unroll
      for (int j = 0; j < 7; ++j) __builtin_amdgcn_global_load_lds((const GAS unsigned*)(x + j * 512 + lane * 8), (LAS unsigned*)(L + X_OFF + j * 1024), 16, 0, 0);
      __builtin_amdgcn_sched_barrier(0);
      DRAIN();
      asm volatile("" : "+v"(eraw));
      {
        unsigned e = eraw;
        e += (unsigned)__builtin_amdgcn_mov_dpp((int)e, XOR1, 0xf, 0xf, true); e += (unsigned)__builtin_amdgcn_mov_dpp((int)e, XOR2, 0xf, 0xf, true);
        e += (unsigned)__builtin_amdgcn_mov_dpp((int)e, XOR7, 0xf, 0xf, true); e += (unsigned)__builtin_amdgcn_mov_dpp((int)e, XOR8, 0xf, 0xf, true);
        tag = 1u + (unsigned)__builtin_amdgcn_readlane((int)e, 0) + (unsigned)__builtin_amdgcn_readlane((int)e, 16) + (unsigned)__builtin_amdgcn_readlane((int)e, 32) + (unsigned)__builtin_amdgcn_readlane((int)e, 48);
      }
      *(volatile LAS v2u*)(L + C_OFF + 8) = v2u{1u, tag};
      v4u xf[14], wf[14];
      LAS unsigned char* wb = L + S_OFF + (8 * g + qq) * 32 + pp * 8;
#pragma unroll
      for (int j = 0; j < 14; ++j) {
        xf[j] = *(const LAS v4u*)(L + X_OFF + (lane & 7) * (D * 2) + 64 * j + 16 * g);
        const v2u lo = trread(wb + j * 1024), hi = trread(wb + j * 1024 + 128); wf[j] = v4u{lo.x, lo.y, hi.x, hi.y};
      }
      __builtin_amdgcn_sched_barrier(0);
      f32x4 d = {0.f, 0.f, 0.f, 0.f}, d2 = {0.f, 0.f, 0.f, 0.f};
#pragma unroll
      for (int j = 0; j < 14; j += 2) { d = mfma(wf[j], xf[j], d); d2 = mfma(wf[j + 1], xf[j + 1], d2); }
      d += d2;
      ST_SC1((v4u{cvtpk(d[0], d[1]), tag, cvtpk(d[2], d[3]), tag}), r16 < 8 ? WS_QG + (r16 * 2048 + ctile * 8 + 2 * g) * 8 : WS_BYTES);
      const int qo = WS_QG + (b * 2048 + kvh * 256) * 8 + lane * 16;
      v4u ga = {0u, 0u, 0u, 0u}, gb = ga;
      SPIN sp < 1024 && (int)(now() - tq0) < QD; ++sp) SLEEP(2);
      WAIT(true, 16) {
        ga = LD_SC1(qo);
        gb = LD_SC1(qo + 1024);
        if (__all(ga.y == tag && ga.w == tag && gb.y == tag && gb.w == tag)) break;
        SLEEP(8);
        FENCE();
      }
      *(LAS v2u*)(L + Q_OFF + lane * 8) = v2u{ga.x, ga.z};
      *(LAS v2u*)(L + Q_OFF + 512 + lane * 8) = v2u{gb.x, gb.z};
      LDSW();
      ctl[10] = 1u;
    } else {
#pragma unroll
      for (int j = 0; j < NPRE; ++j) {
        const int t = (w - 1) + 3 * j;
        wait_full(t + 1);
        LAS unsigned char* tb = L + (t & (NSLOT - 1)) * TILE_B;
#pragma unroll
        for (int i = 0; i < 8; ++i) { pre[j][i] = *(const LAS v4u*)(tb + ka[i & 3] + (i >> 2) * 256); pre[j][8 + i] = *(const LAS v4u*)(tb + ka[i & 3] + (i >> 2) * 256 + 8192); }
        LDSW();
        ctl[4 + w] = (unsigned)(j + 1 < NPRE ? t + 3 : 3 * NPRE + w);
        PIN16(pre[j]);
      }
      WAIT(RFL(ctl[10]) == 0u, 2) { SLEEP(2); }
      FENCE();
      tag = RFL(ctl[3]);
    }
    v4u qf[8];
#pragma unroll
    for (int i = 0; i < 8; ++i) qf[i] = *(const LAS v4u*)(L + Q_OFF + head * 512 + 64 * i + 16 * g);
    float mx = -INFINITY;
    auto qk = [&](const v4u (&a)[16], int t) {
      f32x4 d0 = {0.f, 0.f, 0.f, 0.f}, d1 = {0.f, 0.f, 0.f, 0.f};
#pragma unroll
      for (int i = 0; i < 8; ++i) { d0 = mfma(a[i], qf[i], d0); d1 = mfma(a[8 + i], qf[i], d1); }
      mx = fmaxf(mx, fmaxf(fmaxf(fmaxf(d0[0], d0[1]), fmaxf(d0[2], d0[3])), fmaxf(fmaxf(d1[0], d1[1]), fmaxf(d1[2], d1[3]))));
      if (r16 < 4) {
        const f32x4 d = (r16 & 2) ? d1 : d0;
        *(LAS f32x4*)(L + S_OFF + head * (KPS * 4) + (t * 32 + (r16 & 2) * 8 + 4 * g) * 4) = d;
      }
    };
    if (w > 0) {
#pragma unroll
      for (int j = 0; j < NPRE; ++j) qk(pre[j], (w - 1) + 3 * j);
    }
#pragma nounroll
    for (int t = 3 * NPRE + w; t < NKT; t += NCONS) {
      wait_full(t + 1);
      LAS unsigned char* tb = L + (t & (NSLOT - 1)) * TILE_B;
      v4u a[16];
#pragma unroll
      for (int i = 0; i < 8; ++i) { a[i] = *(const LAS v4u*)(tb + ka[i & 3] + (i >> 2) * 256); a[8 + i] = *(const LAS v4u*)(tb + ka[i & 3] + (i >> 2) * 256 + 8192); }
      LDSW();
      ctl[4 + w] = (unsigned)(t + NCONS < NKT ? t + NCONS : NKT);
      PIN16(a);
      qk(a, t);
    }
    mx = x16max(mx);
    if (lane < 2) ((LAS float*)(L + C_OFF))[16 + w * 2 + lane] = mx;
    LDSW();
    if (lane == 0) __hip_atomic_fetch_add((LAS unsigned*)(L + C_OFF) + 8, 1u, RLX_WG);
    WAIT(RFL(ctl[8]) < NCONS, 1) { SLEEP(1); }
    FENCE();
    constexpr float C2 = 0.0625f * 1.4426950408889634f;
    const f32x4 ma = *(const LAS f32x4*)(L + C_OFF + 64), mb = *(const LAS f32x4*)(L + C_OFF + 80);
    const float M0 = fmaxf(fmaxf(ma[0], ma[2]), fmaxf(mb[0], mb[2])) * C2, M1 = fmaxf(fmaxf(ma[1], ma[3]), fmaxf(mb[1], mb[3])) * C2;
    const int tt = w * 64 + lane, tile = tt >> 2, gg = tt & 3;
    float sum[2];
#pragma unroll
    for (int h = 0; h < 2; ++h) {
      const f32x4 sa = *(const LAS f32x4*)(L + S_OFF + h * (KPS * 4) + (tile * 32 + 4 * gg) * 4);
      const f32x4 sb = *(const LAS f32x4*)(L + S_OFF + h * (KPS * 4) + (tile * 32 + 16 + 4 * gg) * 4);
      const float mc = h ? M1 : M0;
      float p[8];
#pragma unroll
      for (int j = 0; j < 4; ++j) { p[j] = __builtin_amdgcn_exp2f(fmaf(sa[j], C2, -mc)); p[4 + j] = __builtin_amdgcn_exp2f(fmaf(sb[j], C2, -mc)); }
      sum[h] = ((p[0] + p[1]) + (p[2] + p[3])) + ((p[4] + p[5]) + (p[6] + p[7]));
      *(LAS v4u*)(L + P_OFF + tile * 128 + gg * 32 + h * 16) = v4u{cvtpk(p[0], p[1]), cvtpk(p[2], p[3]), cvtpk(p[4], p[5]), cvtpk(p[6], p[7])};
    }
    sum[0] = wavesum(sum[0]); sum[1] = wavesum(sum[1]);
    if (lane < 2) ((LAS float*)(L + C_OFF))[24 + w * 2 + lane] = lane ? sum[1] : sum[0];
    LDSW();
    if (lane == 0) __hip_atomic_fetch_add((LAS unsigned*)(L + C_OFF) + 9, 1u, RLX_WG);
    WAIT(RFL(ctl[9]) < NCONS, 1) { SLEEP(1); }
    FENCE();
    const f32x4 la = *(const LAS f32x4*)(L + C_OFF + 96), lb = *(const LAS f32x4*)(L + C_OFF + 112);
    const float Ls = head ? ((la[1] + la[3]) + (lb[1] + lb[3])) : ((la[0] + la[2]) + (lb[0] + lb[2]));
    oscale = 1.0f / Ls;
    if (w == 1) ST_SC1((v4u{__float_as_uint(head ? M1 : M0), tag, __float_as_uint(Ls), tag}), lane < 2 ? WS_ST + (((kvh * 2 + head) * 8 + b) * 4 + s) * 16 : WS_BYTES);
  }
  const int u = (r16 >> 1) & 3;
  int ooff = r16 < 8 ? WS_PO + ((b * 16 + kvh * 2 + head) * 4 + s) * 1024 + (64 * w + 16 * u + 4 * g) * 4 : WS_BYTES;
  asm volatile("" : "+v"(ooff), "+v"(oscale));
  f32x4 acc[4];
#pragma unroll
  for (int k = 0; k < 4; ++k) acc[k] = f32x4{0.f, 0.f, 0.f, 0.f};
  unsigned tgate = 0;
  {
    const int key = 4 * g + qq;
    unsigned va[4];
#pragma unroll
    for (int k = 0; k < 4; ++k) va[k] = key * 512 + (((8 * w + 2 * k + (pp >> 1)) ^ (2 * (key & 7))) << 4) + (pp & 1) * 8;
#pragma nounroll
    for (int t = 0; t < NKT; ++t) {
      v4u pf = *(const LAS v4u*)(L + P_OFF + t * 128 + g * 32 + head * 16);
      if (t == NKT - 1) tgate = RFL(ctl[13]) + ((NT * TAU) >> 4) + G1;
      wait_full(NKT + t + 1);
      LAS unsigned char* tb = L + (t & (NSLOT - 1)) * TILE_B;
      v4u av[4];
#pragma unroll
      for (int k = 0; k < 4; ++k) { const v2u lo = trread(tb + va[k]), hi = trread(tb + va[k] + 8192); av[k] = v4u{lo.x, lo.y, hi.x, hi.y}; }
      LDSW();
      ctl[4 + w] = (unsigned)(NKT + t + 1);
      asm volatile("" : "+v"(av[0]), "+v"(av[1]), "+v"(av[2]), "+v"(av[3]), "+v"(pf) :: "memory");
#pragma unroll
      for (int k = 0; k < 4; ++k) acc[k] = mfma(av[k], pf, acc[k]);
    }
  }
  {
    const f32x4 a01 = (u & 1) ? acc[1] : acc[0], a23 = (u & 1) ? acc[3] : acc[2];
    const f32x4 a = ((u & 2) ? a23 : a01) * oscale;
    ST_SC1((v4u{cvtpk(a[0], a[1]), tag, cvtpk(a[2], a[3]), tag}), ooff);
  }
  if (!has_out) return;
  const u64 wsa = (u64)ws;
  const v4i srd = {(int)(unsigned)wsa, (int)((unsigned)(wsa >> 32) & 0xffffu), WS_BYTES, 0x00020000};
  v4u p0, p1, p2, p3;
  auto gather_issue = [&](int voff) {
    asm volatile("buffer_load_dwordx4 %0, %4, %5, 0 offen sc1\n\t"
                 "buffer_load_dwordx4 %1, %4, %5, 0 offen offset:1024 sc1\n\t"
                 "buffer_load_dwordx4 %2, %4, %5, 0 offen offset:2048 sc1\n\t"
                 "buffer_load_dwordx4 %3, %4, %5, 0 offen offset:3072 sc1"
                 : "=&v"(p0), "=&v"(p1), "=&v"(p2), "=&v"(p3) : "v"(voff), "s"(srd) : "memory");
  };
  auto gather_wait = [&](int voff) {
    int sp = 0;
    WAIT(true, 16) {
      asm volatile("s_waitcnt vmcnt(0)" : "+v"(p0), "+v"(p1), "+v"(p2), "+v"(p3) :: "memory");
      const unsigned d0 = (p0.y ^ tag) | (p0.w ^ tag), d1 = (p1.y ^ tag) | (p1.w ^ tag), d2 = (p2.y ^ tag) | (p2.w ^ tag), d3 = (p3.y ^ tag) | (p3.w ^ tag);
      if (__builtin_amdgcn_ballot_w64(((d0 | d1) | (d2 | d3)) != 0u) == 0ull) break;
      const u64 m0 = __builtin_amdgcn_ballot_w64(d0 != 0u), m1 = __builtin_amdgcn_ballot_w64(d1 != 0u);
      const u64 m2 = __builtin_amdgcn_ballot_w64(d2 != 0u), m3 = __builtin_amdgcn_ballot_w64(d3 != 0u);
      if (++sp > 16) SLEEP(8);
      asm volatile("s_mov_b64 exec, %5\n\tbuffer_load_dwordx4 %0, %4, %9, 0 offen sc1\n\t"
                   "s_mov_b64 exec, %6\n\tbuffer_load_dwordx4 %1, %4, %9, 0 offen offset:1024 sc1\n\t"
                   "s_mov_b64 exec, %7\n\tbuffer_load_dwordx4 %2, %4, %9, 0 offen offset:2048 sc1\n\t"
                   "s_mov_b64 exec, %8\n\tbuffer_load_dwordx4 %3, %4, %9, 0 offen offset:3072 sc1\n\t"
                   "s_mov_b64 exec, -1"
                   : "+v"(p0), "+v"(p1), "+v"(p2), "+v"(p3) : "v"(voff), "s"(m0), "s"(m1), "s"(m2), "s"(m3), "s"(srd) : "memory");
    }
    asm volatile("s_waitcnt vmcnt(0)" : "+v"(p0), "+v"(p1), "+v"(p2), "+v"(p3) :: "memory");
  };
  {
    int po = WS_PO + ((lane >> 3) * 16 + (rg >> 1)) * 4096 + ((rg & 1) * 128 + 32 * w) * 4 + (lane & 7) * 16;
    const int so = WS_ST + (rg >> 1) * 512 + (lane & 31) * 16;
    v4u sc;
    asm volatile("buffer_load_dwordx4 %0, %2, %3, 0 offen sc1" : "=&v"(sc), "+v"(po) : "v"(so), "s"(srd) : "memory");
    SPIN sp < 32 && (int)(now() - tgate) < 0; ++sp) SLEEP(1);
    gather_issue(po);
    asm volatile("s_waitcnt vmcnt(4)" : "+v"(sc) :: "memory");
    WAIT(!__all(sc.y == tag && sc.w == tag), 16) {
      SLEEP(8);
      asm volatile("buffer_load_dwordx4 %0, %1, %2, 0 offen sc1\n\ts_waitcnt vmcnt(0)" : "=&v"(sc) : "v"(so), "s"(srd) : "memory");
    }
    f32x4 wya, wyb;
    {
      const float ms = __uint_as_float(sc.x), ls = __uint_as_float(sc.z);
      float M = fmaxf(ms, dpp<XOR1>(ms)); M = fmaxf(M, dpp<XOR2>(M));
      const float e = __builtin_amdgcn_exp2f(ms - M) * ls;
      float Lt = e + dpp<XOR1>(e); Lt += dpp<XOR2>(Lt);
      LAS unsigned char* qb = L + Q_OFF + w * 128;
      *(LAS float*)(qb + (lane & 3) * 32 + ((lane >> 2) & 7) * 4) = e / Lt;
      LDSW();
      wya = *(const LAS f32x4*)(qb + g * 16); wyb = *(const LAS f32x4*)(qb + 64 + g * 16);
    }
    WAIT(RFL(ctl[11]) == 0u, 1) { SLEEP(1); }
    FENCE();
    v4u wa[4];
    {
      LAS unsigned char* wb = L + S_OFF + (8 * g + qq) * 128 + w * 32 + pp * 8;
#pragma unroll
      for (int k = 0; k < 4; ++k) { const v2u lo = trread(wb + k * 4096), hi = trread(wb + k * 4096 + 512); wa[k] = v4u{lo.x, lo.y, hi.x, hi.y}; }
    }
    int yo = WS_Y2 + ((((g & 1) * 4 + (g >> 1)) * 7 + lc) * 4 + w) * 4096 + rg * 128 + r16 * 8, yo2 = yo + 2 * 7 * 4 * 4096;
    LAS unsigned char* xb = L + X_OFF + w * 2048 + lane * 8;
    LAS unsigned char* xr = L + X_OFF + r16 * 64 + g * 16;
    asm volatile("" : "+v"(yo), "+v"(yo2), "+v"(xb), "+v"(xr));
    gather_wait(po);
    *(LAS v2u*)(xb) = v2u{p0.x, p0.z}; *(LAS v2u*)(xb + 512) = v2u{p1.x, p1.z};
    *(LAS v2u*)(xb + 1024) = v2u{p2.x, p2.z}; *(LAS v2u*)(xb + 1536) = v2u{p3.x, p3.z};
    LDSW();
    __builtin_amdgcn_s_barrier();
    FENCE();
    v4u oa[8];
#pragma unroll
    for (int k = 0; k < 8; ++k) oa[k] = *(const LAS v4u*)(xr + k * 1024);
    __builtin_amdgcn_sched_barrier(0);
    f32x4 ya = {0.f, 0.f, 0.f, 0.f}, yb = {0.f, 0.f, 0.f, 0.f};
#pragma unroll
    for (int k = 0; k < 4; ++k) { ya = mfma(oa[2 * k], wa[k], ya); yb = mfma(oa[2 * k + 1], wa[k], yb); }
    ya = ya * wya + yb * wyb;
    auto t0 = __builtin_amdgcn_permlane32_swap(__float_as_uint(ya[0]), __float_as_uint(ya[1]), false, false);
    auto t1 = __builtin_amdgcn_permlane32_swap(__float_as_uint(ya[2]), __float_as_uint(ya[3]), false, false);
    const float v0 = __uint_as_float(t0[0]) + __uint_as_float(t0[1]), v1 = __uint_as_float(t1[0]) + __uint_as_float(t1[1]);
    __builtin_amdgcn_raw_buffer_store_b64(v2u{__float_as_uint(v0), tag}, rs, yo, 0, 16);
    __builtin_amdgcn_raw_buffer_store_b64(v2u{__float_as_uint(v1), tag}, rs, yo2, 0, 16);
  }
  if (w != 0) return;
  {
    const int nt = (bid >> 3) & 3, b3 = bid & 7;
    const int yo = WS_Y2 + ((b3 * 7 + lc) * 4 + nt) * 4096 + lane * 16;
    const __amdgpu_buffer_rsrc_t ro = __builtin_amdgcn_make_buffer_rsrc((void*)out, 0, B * D * 2, 0x00020000);
    int oo = lane < 8 ? (b3 * D + lc * 64 + nt * 16 + 2 * lane) * 2 : B * D * 2;
    asm volatile("" : "+v"(oo));
    SLEEP(D2);
    gather_issue(yo);
    gather_wait(yo);
    float v0 = (__uint_as_float(p0.x) + __uint_as_float(p1.x)) + (__uint_as_float(p2.x) + __uint_as_float(p3.x));
    float v1 = (__uint_as_float(p0.z) + __uint_as_float(p1.z)) + (__uint_as_float(p2.z) + __uint_as_float(p3.z));
    v0 += dpp<XOR8>(v0); v1 += dpp<XOR8>(v1);
    v0 = x16sum(v0); v1 = x16sum(v1);
    __builtin_amdgcn_raw_buffer_store_b32(cvtpk(v0, v1), ro, oo, 0, 0);
    __builtin_amdgcn_raw_buffer_store_b32(eraw + 1u, rs, bid == 0 ? eoff : WS_BYTES, 0, 16);
  }
}

extern "C" void kernel_launch(void* const* d_in, const int* in_sizes, int n_in, void* d_out, int out_size, void* d_ws, size_t ws_size, hipStream_t stream) {
  (void)hipFuncSetAttribute((const void*)k_fused, hipFuncAttributeMaxDynamicSharedMemorySize, LDS_BYTES);
  k_fused<<<256, NWAVE * 64, LDS_BYTES, stream>>>((const unsigned short*)d_in[0], (const unsigned short*)d_in[1], (const unsigned short*)d_in[2],
      (const unsigned short*)d_in[3], (const unsigned short*)d_in[4], (unsigned short*)d_out, (unsigned char*)d_ws);
}
```

```cpp
#include <hip/hip_runtime.h>
#include <hip/hip_bf16.h>
#include <stdint.h>

typedef unsigned v4u __attribute__((ext_vector_type(4)));
typedef unsigned v2u __attribute__((ext_vector_type(2)));
typedef int v4i __attribute__((ext_vector_type(4)));
typedef float f32x4 __attribute__((ext_vector_type(4)));
typedef float f32x2 __attribute__((ext_vector_type(2)));
typedef short s16x4 __attribute__((ext_vector_type(4)));
typedef __bf16 bf16x8 __attribute__((ext_vector_type(8)));
typedef __bf16 bf16x2 __attribute__((ext_vector_type(2)));
typedef unsigned long long u64;
#define LAS __attribute__((address_space(3)))
#define GAS __attribute__((address_space(1)))
#define RLX_AGENT __ATOMIC_RELAXED, __HIP_MEMORY_SCOPE_AGENT
#define RLX_WG __ATOMIC_RELAXED, __HIP_MEMORY_SCOPE_WORKGROUP

constexpr int B = 8, D = 448, DH = 256;
constexpr int KPS = 2048;
#define TAU 880
#define LEAD 30
#define TH1 40
#define TH2 150
#define PK0 8
#define PKG 24
#define PKD 3
#define PFD 32
#define QD 550
#define WO_AT 127
#define G1 100
#define GOWN 30
#define D2 15
constexpr int CODE_SPAN = 13312;
constexpr int NPRE = 3;
constexpr int TILE_B = 16384, NSLOT = 8, NKT = KPS / 32, NT = 2 * NKT;
constexpr int NCONS = 4, NWAVE = 6;
constexpr int WAIT_BUDGET = 1 << 22;
constexpr int S_OFF = NSLOT * TILE_B;
constexpr int X_OFF = S_OFF + 2 * KPS * 4;
constexpr int P_OFF = X_OFF + 2048;
constexpr int Q_OFF = P_OFF + NKT * 128;
constexpr int C_OFF = Q_OFF + 1024;
constexpr int Z_OFF = C_OFF + 256;
constexpr int LDS_BYTES = Z_OFF + 1024;
constexpr int WS_QG = 4096;
constexpr int WS_ST = WS_QG + 8 * 2048 * 8 + 1024;
constexpr int WS_PO = WS_ST + 16 * 8 * 4 * 16 + 1024;
constexpr int WS_Y2 = WS_PO + 8 * 16 * 4 * 1024 + 1024;
constexpr int WS_PH = WS_Y2 + 8 * 7 * 4 * 4096 + 1024;
constexpr int WS_BYTES = WS_PH + 256 * 128 + 1024;

__device__ __forceinline__ unsigned cvtpk(float lo, float hi) { f32x2 v = {lo, hi}; return __builtin_bit_cast(unsigned, __builtin_convertvector(v, bf16x2)); }
__device__ __forceinline__ f32x4 mfma(v4u a, v4u b, f32x4 c) {
  return __builtin_amdgcn_mfma_f32_16x16x32_bf16(__builtin_bit_cast(bf16x8, a), __builtin_bit_cast(bf16x8, b), c, 0, 0, 0);
}
__device__ __forceinline__ v2u trread(LAS unsigned char* p) { return __builtin_bit_cast(v2u, __builtin_amdgcn_ds_read_tr16_b64_v4i16((LAS s16x4*)p)); }
__device__ __forceinline__ float x16max(float x) {
  auto s = __builtin_amdgcn_permlane16_swap(__float_as_uint(x), __float_as_uint(x), false, false);
  x = fmaxf(__uint_as_float(s[0]), __uint_as_float(s[1]));
  auto t = __builtin_amdgcn_permlane32_swap(__float_as_uint(x), __float_as_uint(x), false, false);
  return fmaxf(__uint_as_float(t[0]), __uint_as_float(t[1]));
}
__device__ __forceinline__ float x16sum(float x) {
  auto s = __builtin_amdgcn_permlane16_swap(__float_as_uint(x), __float_as_uint(x), false, false);
  x = __uint_as_float(s[0]) + __uint_as_float(s[1]);
  auto t = __builtin_amdgcn_permlane32_swap(__float_as_uint(x), __float_as_uint(x), false, false);
  return __uint_as_float(t[0]) + __uint_as_float(t[1]);
}
template <int CTRL> __device__ __forceinline__ float dpp(float x) {
  return __builtin_bit_cast(float, __builtin_amdgcn_mov_dpp(__builtin_bit_cast(int, x), CTRL, 0xf, 0xf, true));
}
constexpr int XOR1 = 0xB1, XOR2 = 0x4E, XOR7 = 0x141, XOR8 = 0x128;
__device__ __forceinline__ float wavesum(float x) {
  x += dpp<XOR1>(x); x += dpp<XOR2>(x); x += dpp<XOR7>(x); x += dpp<XOR8>(x);
  return x16sum(x);
}
__device__ __forceinline__ unsigned now() { return (unsigned)__builtin_amdgcn_s_memrealtime(); }
#define LDSW() asm volatile("s_waitcnt lgkmcnt(0)" ::: "memory")
#define DRAIN() asm volatile("s_waitcnt vmcnt(0)" ::: "memory")
#define FENCE() asm volatile("" ::: "memory")
#define PIN16(a) asm volatile("" : "+v"(a[0]), "+v"(a[1]), "+v"(a[2]), "+v"(a[3]), "+v"(a[4]), "+v"(a[5]), "+v"(a[6]), "+v"(a[7]), \
                        "+v"(a[8]), "+v"(a[9]), "+v"(a[10]), "+v"(a[11]), "+v"(a[12]), "+v"(a[13]), "+v"(a[14]), "+v"(a[15]) :: "memory")
#define RFL(x) __builtin_amdgcn_readfirstlane(x)
#define SLEEP(n) __builtin_amdgcn_s_sleep(n)
#define LD_SC1(off) __builtin_amdgcn_raw_buffer_load_b128(rs, (off), 0, 16)
#define ST_SC1(v, off) __builtin_amdgcn_raw_buffer_store_b128((v), rs, (off), 0, 16)
#define SPIN _Pragma("nounroll") for (int sp = 0;
#define WAIT(cond, cost) _Pragma("nounroll") for (; bud > 0 && (cond); bud -= (cost))

__global__ void __launch_bounds__(NWAVE * 64, 2) k_fused(const unsigned short* __restrict__ x, const unsigned short* __restrict__ kc, const unsigned short* __restrict__ vc,
                                              const unsigned short* __restrict__ wq, const unsigned short* __restrict__ wo, unsigned short* __restrict__ out, unsigned char* ws) {
  extern __shared__ __attribute__((aligned(16))) unsigned char lds[];
  LAS unsigned char* L = (LAS unsigned char*)lds;
  volatile LAS unsigned* ctl = (volatile LAS unsigned*)(L + C_OFF);
  const int bid = blockIdx.x;
  const int lane = threadIdx.x & 63, wave = RFL(threadIdx.x >> 6);
  int bud = WAIT_BUDGET;
  {
    unsigned t0_, v0_;
    asm volatile("s_lshr_b32 %0, %3, 4\n\ts_lshl3_add_u32 %0, %0, %4\n\ts_cmp_lg_u32 %0, 5\n\ts_cbranch_scc1 .Lskip%=\n\t"
                 "s_getpc_b64 s[60:61]\n\ts_and_b32 s60, s60, 0xffffff00\n\t"
                 "v_and_b32 %1, 63, %2\n\tv_min_u32 %1, %6, %1\n\tv_lshlrev_b32 %1, 8, %1\n\t"
                 "s_mov_b32 m0, %5\n\ts_nop 0\n\t"
                 "global_load_lds_dwordx4 %1, s[60:61] offset:120\n"
                 ".Lskip%=:"
                 : "=&s"(t0_), "=&v"(v0_) : "v"(threadIdx.x), "s"(blockIdx.x), "s"(wave), "i"(Z_OFF - 120), "i"(CODE_SPAN / 256 - 1) : "memory", "scc", "s60", "s61");
  }
  u64 src = (u64)kc + (u64)bid * (KPS * DH * 2);
#define DMA4(S) "global_load_lds_dwordx4 %1, " S " nt\n\tglobal_load_lds_dwordx4 %2, " S " offset:1024 nt\n\t" \
                "global_load_lds_dwordx4 %3, " S " offset:2048 nt\n\tglobal_load_lds_dwordx4 %4, " S " offset:3072 nt\n\t"
  unsigned o0, o1, o2, o3;
  {
    unsigned t_;
    asm volatile("s_cmp_lg_u32 %7, 0\n\ts_cbranch_scc1 .Lskip%=\n\t"
                 "s_setprio 3\n\t"
                 "v_lshlrev_b32 %1, 4, %5\n\tv_and_b32 %0, 32, %5\n\tv_xor_b32 %1, %1, %0\n\t"
                 "v_xor_b32 %2, 64, %1\n\tv_xor_b32 %3, 0x80, %1\n\tv_xor_b32 %4, 64, %3\n\t"
                 "s_mov_b32 m0, 0\n\ts_mov_b64 s[60:61], %6\n"
                 ".Lloop%=:\n\t" DMA4("s[60:61]")
                 "s_add_u32 m0, m0, 0x1000\n\t"
                 "s_add_u32 s60, s60, 0x1000\n\ts_addc_u32 s61, s61, 0\n\t"
                 "s_cmpk_lg_u32 m0, 0x8000\n\ts_cbranch_scc1 .Lloop%=\n"
                 ".Lskip%=:"
                 : "=&v"(t_), "=&v"(o0), "=&v"(o1), "=&v"(o2), "=&v"(o3)
                 : "v"(threadIdx.x), "s"(src), "s"(wave) : "memory", "scc", "s60", "s61");
  }
  const unsigned ldsbase = (unsigned)(size_t)L;
  if (threadIdx.x < 64) ((LAS unsigned*)(L + C_OFF))[lane] = 0u;
  LDSW();
  __builtin_amdgcn_s_barrier();
  if (wave == 0) {
    const u64 vb = (u64)vc + (u64)bid * (KPS * DH * 2);
    const unsigned xm = (unsigned)(bid & 1) * TILE_B;
    int pub = 0;
    unsigned phi = 0;
#pragma nounroll
    for (int t = 2; t < NT + 3; ++t) {
      if (t >= NSLOT && t < NT) {
        const unsigned needt = (unsigned)(t - NSLOT);
        WAIT(true, 1) {
          const v4u nd = *(volatile LAS v4u*)(L + C_OFF + 16);
          const unsigned mn = min(min(nd.x, nd.y), min(nd.z, nd.w));
          if (RFL(mn) > needt) break;
          SLEEP(1);
        }
        FENCE();
      }
      const v2u rf = *(volatile LAS v2u*)(L + C_OFF + 48);
      const unsigned rvalid = RFL(rf.x), ref = RFL(rf.y);
      const int e = t >= NT ? (t == NT ? TH2 + 1 : t == NT + 1 ? TH1 + 1 : 0) : rvalid ? (int)(phi - ref) : 0;
      const unsigned tt = ((unsigned)t * TAU) >> 4;
      const int issued = t < NT ? t : NT;
      if (t == NKT) src = vb;
      int landed; unsigned adj;
      if (e > TH2) { asm volatile("s_waitcnt vmcnt(47)" ::: "memory"); landed = issued - 3; adj = (31 * TAU) >> 8; }
      else if (e > TH1) { asm volatile("s_waitcnt vmcnt(32)" ::: "memory"); landed = issued - 2; adj = TAU >> 4; }
      else {
        asm volatile("s_waitcnt vmcnt(16)" ::: "memory"); landed = issued - 1; adj = 0;
        if (e < 0) { const unsigned due = ref + tt - LEAD; SPIN sp < 64 && (int)(now() - due) < 0; ++sp) SLEEP(1); }
      }
      if (t < NT) {
        const unsigned dst = ldsbase + (t & (NSLOT - 1)) * TILE_B;
        const unsigned ti = (unsigned)(t & (NKT - 1));
        const u64 s0 = src + ((ti * TILE_B) ^ (ti < 2 ? 0u : xm));
        const u64 s1 = s0 + 4096, s2 = s0 + 8192, s3 = s0 + 12288;
        unsigned keep;
        asm volatile("s_mov_b32 %0, m0\n\t"
                     "s_mov_b32 m0, %9\n\ts_nop 0\n\t" DMA4("%5")
                     "s_add_u32 m0, %9, 0x1000\n\ts_nop 0\n\t" DMA4("%6")
                     "s_add_u32 m0, %9, 0x2000\n\ts_nop 0\n\t" DMA4("%7")
                     "s_add_u32 m0, %9, 0x3000\n\ts_nop 0\n\t" DMA4("%8")
                     "s_mov_b32 m0, %0"
                     : "=&s"(keep) : "v"(o0), "v"(o1), "v"(o2), "v"(o3), "s"(s0), "s"(s1), "s"(s2), "s"(s3), "s"(dst) : "memory", "scc");
      }
      if (landed > pub) pub = landed;
      phi = now() - tt + adj;
      *(volatile LAS v2u*)(L + C_OFF) = v2u{(unsigned)pub, phi};
    }
    DRAIN();
    ctl[0] = (unsigned)NT;
    return;
  }
  const int s = bid & 3, kvh = (bid >> 2) & 7, b = bid >> 5;
  const bool has_out = (bid >> 5) < 7;
  const int lc = has_out ? (bid >> 5) : 0, rg = (bid & 7) * 4 + ((bid >> 3) & 3);
  auto wait_full = [&](unsigned n) { WAIT(RFL(ctl[0]) < n, 1) { SLEEP(1); } FENCE(); };
  auto wait_full_slow = [&](unsigned n) { WAIT(RFL(ctl[0]) < n, 4) { SLEEP(4); } FENCE(); };
  if (wave == 5) {
    WAIT(RFL(ctl[2]) == 0u, 4) { SLEEP(4); }
    const unsigned tag = RFL(ctl[3]);
    int mixs = 4;
#pragma nounroll
    for (int k = PK0; k + PKD < WO_AT; k += PKG) {
      wait_full_slow(k);
      const unsigned mine = RFL(ctl[1]);
      if (lane == 0) __hip_atomic_store((GAS u64*)(ws + WS_PH) + bid * 16, ((u64)tag << 32) | mine, RLX_AGENT);
      wait_full_slow(k + PKD);
      u64 o = 0;
      if (lane < 8) o = __hip_atomic_load((GAS u64*)(ws + WS_PH) + (((((bid >> 3) + mixs * lane) & 31) << 3) + lane) * 16, RLX_AGENT);
      mixs = 5 - mixs;
      const bool ok = (unsigned)(o >> 32) == tag && lane < 8;
      const float dsum = wavesum(ok ? (float)(int)((unsigned)o - mine) : 0.f);
      const int n = __builtin_popcountll(__builtin_amdgcn_ballot_w64(ok));
      if (n >= 4) { const unsigned ref = mine + (unsigned)(int)(dsum / (float)n); *(volatile LAS v2u*)(L + C_OFF + 48) = v2u{1u, ref}; }
    }
    if (!has_out) return;
    const unsigned char* wsrc = (const unsigned char*)wo + (size_t)(rg * 128 + (lane >> 3)) * (D * 2) + lc * 128 + (lane & 7) * 16;
    wait_full(WO_AT);
    WAIT(RFL(ctl[9]) < NCONS, 4) { SLEEP(4); }
    FENCE();
#pragma unroll
    for (int m = 0; m < 16; ++m) __builtin_amdgcn_global_load_lds((const GAS unsigned*)(wsrc + m * 8 * (D * 2)), (LAS unsigned*)(L + S_OFF + m * 1024), 16, 0, 2);
    DRAIN();
    ctl[11] = 1u;
    return;
  }
  const __amdgpu_buffer_rsrc_t rs = __builtin_amdgcn_make_buffer_rsrc((void*)ws, 0, WS_BYTES, 0x00020000);
  const int w = wave - 1;
  const int g = lane >> 4, r16 = lane & 15, head = lane & 1;
  const int qq = (lane >> 2) & 3, pp = lane & 3;
  unsigned tag, eraw = 0;
  int eoff = WS_BYTES;
  float oscale;
  {
    unsigned ka[4];
#pragma unroll
    for (int i = 0; i < 4; ++i) ka[i] = r16 * 512 + (((4 * i + g) ^ (2 * (r16 & 7))) << 4);
    v4u pre[NPRE][16];
    if (w == 0) {
      const unsigned tq0 = now();
      ctl[4] = (unsigned)(3 * NPRE);
      {
        const int r = (lane & 7) < 7 ? (lane & 7) : (lane >> 3), word = (lane & 7) < 7 ? (lane >> 3) : 8;
        const int end = r == 0 ? 128 : r == 1 ? WS_QG : r == 2 ? WS_ST : r == 3 ? WS_PO : r == 4 ? WS_Y2 : r == 5 ? WS_PH : WS_BYTES;
        eoff = r == 7 ? WS_BYTES : end - 128 + word * 4;
      }
      const int ctile = (bid & 7) * 32 + (bid >> 3);
      {
        const unsigned short* ps = wq + (size_t)((bid >> 3) * 14) * 4096 + (bid & 7) * 512 + lane * 8;
#pragma unroll
        for (int j = 0; j < 14; ++j) __builtin_amdgcn_global_load_lds((const GAS unsigned*)(ps + (size_t)j * 4096), (LAS unsigned*)(L + S_OFF + j * 1024), 16, 0, 0);
        DRAIN();
        eraw = __builtin_amdgcn_raw_buffer_load_b32(rs, eoff, 0, 16);
        __builtin_amdgcn_sched_barrier(0);
        SLEEP(PFD);
      }
      const unsigned short* wsrc = wq + (size_t)(lane >> 1) * 4096 + ctile * 16 + (lane & 1) * 8;
#pragma unroll
      for (int j = 0; j < 14; ++j) __builtin_amdgcn_global_load_lds((const GAS unsigned*)(wsrc + (size_t)j * 32 * 4096), (LAS unsigned*)(L + S_OFF + j * 1024), 16, 0, 2);
#pragma unroll
      for (int j = 0; j < 7; ++j) __builtin_amdgcn_global_load_lds((const GAS unsigned*)(x + j * 512 + lane * 8), (LAS unsigned*)(L + X_OFF + j * 1024), 16, 0, 0);
      __builtin_amdgcn_sched_barrier(0);
      DRAIN();
      asm volatile("" : "+v"(eraw));
      {
        unsigned e = eraw;
        e += (unsigned)__builtin_amdgcn_mov_dpp((int)e, XOR1, 0xf, 0xf, true); e += (unsigned)__builtin_amdgcn_mov_dpp((int)e, XOR2, 0xf, 0xf, true);
        e += (unsigned)__builtin_amdgcn_mov_dpp((int)e, XOR7, 0xf, 0xf, true); e += (unsigned)__builtin_amdgcn_mov_dpp((int)e, XOR8, 0xf, 0xf, true);
        tag = 1u + (unsigned)__builtin_amdgcn_readlane((int)e, 0) + (unsigned)__builtin_amdgcn_readlane((int)e, 16) + (unsigned)__builtin_amdgcn_readlane((int)e, 32) + (unsigned)__builtin_amdgcn_readlane((int)e, 48);
      }
      *(volatile LAS v2u*)(L + C_OFF + 8) = v2u{1u, tag};
      v4u xf[14], wf[14];
      LAS unsigned char* wb = L + S_OFF + (8 * g + qq) * 32 + pp * 8;
#pragma unroll
      for (int j = 0; j < 14; ++j) {
        xf[j] = *(const LAS v4u*)(L + X_OFF + (lane & 7) * (D * 2) + 64 * j + 16 * g);
        const v2u lo = trread(wb + j * 1024), hi = trread(wb + j * 1024 + 128); wf[j] = v4u{lo.x, lo.y, hi.x, hi.y};
      }
      __builtin_amdgcn_sched_barrier(0);
      f32x4 d = {0.f, 0.f, 0.f, 0.f}, d2 = {0.f, 0.f, 0.f, 0.f};
#pragma unroll
      for (int j = 0; j < 14; j += 2) { d = mfma(wf[j], xf[j], d); d2 = mfma(wf[j + 1], xf[j + 1], d2); }
      d += d2;
      ST_SC1((v4u{cvtpk(d[0], d[1]), tag, cvtpk(d[2], d[3]), tag}), r16 < 8 ? WS_QG + (r16 * 2048 + ctile * 8 + 2 * g) * 8 : WS_BYTES);
      const int qo = WS_QG + (b * 2048 + kvh * 256) * 8 + lane * 16;
      v4u ga = {0u, 0u, 0u, 0u}, gb = ga;
      SPIN sp < 1024 && (int)(now() - tq0) < QD; ++sp) SLEEP(2);
      WAIT(true, 16) {
        ga = LD_SC1(qo);
        gb = LD_SC1(qo + 1024);
        if (__all(ga.y == tag && ga.w == tag && gb.y == tag && gb.w == tag)) break;
        SLEEP(8);
        FENCE();
      }
      *(LAS v2u*)(L + Q_OFF + lane * 8) = v2u{ga.x, ga.z};
      *(LAS v2u*)(L + Q_OFF + 512 + lane * 8) = v2u{gb.x, gb.z};
      LDSW();
      ctl[10] = 1u;
    } else {
#pragma unroll
      for (int j = 0; j < NPRE; ++j) {
        const int t = (w - 1) + 3 * j;
        wait_full(t + 1);
        LAS unsigned char* tb = L + (t & (NSLOT - 1)) * TILE_B;
#pragma unroll
        for (int i = 0; i < 8; ++i) { pre[j][i] = *(const LAS v4u*)(tb + ka[i & 3] + (i >> 2) * 256); pre[j][8 + i] = *(const LAS v4u*)(tb + ka[i & 3] + (i >> 2) * 256 + 8192); }
        LDSW();
        ctl[4 + w] = (unsigned)(j + 1 < NPRE ? t + 3 : 3 * NPRE + w);
        PIN16(pre[j]);
      }
      WAIT(RFL(ctl[10]) == 0u, 2) { SLEEP(2); }
      FENCE();
      tag = RFL(ctl[3]);
    }
    v4u qf[8];
#pragma unroll
    for (int i = 0; i < 8; ++i) qf[i] = *(const LAS v4u*)(L + Q_OFF + head * 512 + 64 * i + 16 * g);
    float mx = -INFINITY;
    auto qk = [&](const v4u (&a)[16], int t) {
      f32x4 d0 = {0.f, 0.f, 0.f, 0.f}, d1 = {0.f, 0.f, 0.f, 0.f};
#pragma unroll
      for (int i = 0; i < 8; ++i) { d0 = mfma(a[i], qf[i], d0); d1 = mfma(a[8 + i], qf[i], d1); }
      mx = fmaxf(mx, fmaxf(fmaxf(fmaxf(d0[0], d0[1]), fmaxf(d0[2], d0[3])), fmaxf(fmaxf(d1[0], d1[1]), fmaxf(d1[2], d1[3]))));
      if (r16 < 4) {
        const f32x4 d = (r16 & 2) ? d1 : d0;
        *(LAS f32x4*)(L + S_OFF + head * (KPS * 4) + (t * 32 + (r16 & 2) * 8 + 4 * g) * 4) = d;
      }
    };
    if (w > 0) {
#pragma unroll
      for (int j = 0; j < NPRE; ++j) qk(pre[j], (w - 1) + 3 * j);
    }
#pragma nounroll
    for (int t = 3 * NPRE + w; t < NKT; t += NCONS) {
      wait_full(t + 1);
      LAS unsigned char* tb = L + (t & (NSLOT - 1)) * TILE_B;
      v4u a[16];
#pragma unroll
      for (int i = 0; i < 8; ++i) { a[i] = *(const LAS v4u*)(tb + ka[i & 3] + (i >> 2) * 256); a[8 + i] = *(const LAS v4u*)(tb + ka[i & 3] + (i >> 2) * 256 + 8192); }
      LDSW();
      ctl[4 + w] = (unsigned)(t + NCONS < NKT ? t + NCONS : NKT);
      PIN16(a);
      qk(a, t);
    }
    mx = x16max(mx);
    if (lane < 2) ((LAS float*)(L + C_OFF))[16 + w * 2 + lane] = mx;
    LDSW();
    if (lane == 0) __hip_atomic_fetch_add((LAS unsigned*)(L + C_OFF) + 8, 1u, RLX_WG);
    WAIT(RFL(ctl[8]) < NCONS, 1) { SLEEP(1); }
    FENCE();
    constexpr float C2 = 0.0625f * 1.4426950408889634f;
    const f32x4 ma = *(const LAS f32x4*)(L + C_OFF + 64), mb = *(const LAS f32x4*)(L + C_OFF + 80);
    const float M0 = fmaxf(fmaxf(ma[0], ma[2]), fmaxf(mb[0], mb[2])) * C2, M1 = fmaxf(fmaxf(ma[1], ma[3]), fmaxf(mb[1], mb[3])) * C2;
    const int tt = w * 64 + lane, tile = tt >> 2, gg = tt & 3;
    float sum[2];
#pragma unroll
    for (int h = 0; h < 2; ++h) {
      const f32x4 sa = *(const LAS f32x4*)(L + S_OFF + h * (KPS * 4) + (tile * 32 + 4 * gg) * 4);
      const f32x4 sb = *(const LAS f32x4*)(L + S_OFF + h * (KPS * 4) + (tile * 32 + 16 + 4 * gg) * 4);
      const float mc = h ? M1 : M0;
      float p[8];
#pragma unroll
      for (int j = 0; j < 4; ++j) { p[j] = __builtin_amdgcn_exp2f(fmaf(sa[j], C2, -mc)); p[4 + j] = __builtin_amdgcn_exp2f(fmaf(sb[j], C2, -mc)); }
      sum[h] = ((p[0] + p[1]) + (p[2] + p[3])) + ((p[4] + p[5]) + (p[6] + p[7]));
      *(LAS v4u*)(L + P_OFF + tile * 128 + gg * 32 + h * 16) = v4u{cvtpk(p[0], p[1]), cvtpk(p[2], p[3]), cvtpk(p[4], p[5]), cvtpk(p[6], p[7])};
    }
    sum[0] = wavesum(sum[0]); sum[1] = wavesum(sum[1]);
    if (lane < 2) ((LAS float*)(L + C_OFF))[24 + w * 2 + lane] = lane ? sum[1] : sum[0];
    LDSW();
    if (lane == 0) __hip_atomic_fetch_add((LAS unsigned*)(L + C_OFF) + 9, 1u, RLX_WG);
    WAIT(RFL(ctl[9]) < NCONS, 1) { SLEEP(1); }
    FENCE();
    const f32x4 la = *(const LAS f32x4*)(L + C_OFF + 96), lb = *(const LAS f32x4*)(L + C_OFF + 112);
    const float Ls = head ? ((la[1] + la[3]) + (lb[1] + lb[3])) : ((la[0] + la[2]) + (lb[0] + lb[2]));
    oscale = 1.0f / Ls;
    if (w == 1) ST_SC1((v4u{__float_as_uint(head ? M1 : M0), tag, __float_as_uint(Ls), tag}), lane < 2 ? WS_ST + (((kvh * 2 + head) * 8 + b) * 4 + s) * 16 : WS_BYTES);
  }
  const int u = (r16 >> 1) & 3;
  int ooff = r16 < 8 ? WS_PO + ((b * 16 + kvh * 2 + head) * 4 + s) * 1024 + (64 * w + 16 * u + 4 * g) * 4 : WS_BYTES;
  asm volatile("" : "+v"(ooff), "+v"(oscale));
  f32x4 acc[4];
#pragma unroll
  for (int k = 0; k < 4; ++k) acc[k] = f32x4{0.f, 0.f, 0.f, 0.f};
  unsigned tgate = 0;
  {
    const int key = 4 * g + qq;
    unsigned va[4];
#pragma unroll
    for (int k = 0; k < 4; ++k) va[k] = key * 512 + (((8 * w + 2 * k + (pp >> 1)) ^ (2 * (key & 7))) << 4) + (pp & 1) * 8;
#pragma nounroll
    for (int t = 0; t < NKT; ++t) {
      v4u pf = *(const LAS v4u*)(L + P_OFF + t * 128 + g * 32 + head * 16);
      if (t == NKT - 1) tgate = RFL(ctl[13]) + ((NT * TAU) >> 4) + G1;
      wait_full(NKT + t + 1);
      LAS unsigned char* tb = L + (t & (NSLOT - 1)) * TILE_B;
      v4u av[4];
#pragma unroll
      for (int k = 0; k < 4; ++k) { const v2u lo = trread(tb + va[k]), hi = trread(tb + va[k] + 8192); av[k] = v4u{lo.x, lo.y, hi.x, hi.y}; }
      LDSW();
      ctl[4 + w] = (unsigned)(NKT + t + 1);
      asm volatile("" : "+v"(av[0]), "+v"(av[1]), "+v"(av[2]), "+v"(av[3]), "+v"(pf) :: "memory");
#pragma unroll
      for (int k = 0; k < 4; ++k) acc[k] = mfma(av[k], pf, acc[k]);
    }
  }
  {
    const f32x4 a01 = (u & 1) ? acc[1] : acc[0], a23 = (u & 1) ? acc[3] : acc[2];
    const f32x4 a = ((u & 2) ? a23 : a01) * oscale;
    ST_SC1((v4u{cvtpk(a[0], a[1]), tag, cvtpk(a[2], a[3]), tag}), ooff);
  }
  if (!has_out) return;
  const u64 wsa = (u64)ws;
  const v4i srd = {(int)(unsigned)wsa, (int)((unsigned)(wsa >> 32) & 0xffffu), WS_BYTES, 0x00020000};
  v4u p0, p1, p2, p3;
  auto gather_issue = [&](int voff) {
    asm volatile("buffer_load_dwordx4 %0, %4, %5, 0 offen sc1\n\t"
                 "buffer_load_dwordx4 %1, %4, %5, 0 offen offset:1024 sc1\n\t"
                 "buffer_load_dwordx4 %2, %4, %5, 0 offen offset:2048 sc1\n\t"
                 "buffer_load_dwordx4 %3, %4, %5, 0 offen offset:3072 sc1"
                 : "=&v"(p0), "=&v"(p1), "=&v"(p2), "=&v"(p3) : "v"(voff), "s"(srd) : "memory");
  };
  auto gather_wait = [&](int voff) {
    int sp = 0;
    WAIT(true, 16) {
      asm volatile("s_waitcnt vmcnt(0)" : "+v"(p0), "+v"(p1), "+v"(p2), "+v"(p3) :: "memory");
      const unsigned d0 = (p0.y ^ tag) | (p0.w ^ tag), d1 = (p1.y ^ tag) | (p1.w ^ tag), d2 = (p2.y ^ tag) | (p2.w ^ tag), d3 = (p3.y ^ tag) | (p3.w ^ tag);
      if (__builtin_amdgcn_ballot_w64(((d0 | d1) | (d2 | d3)) != 0u) == 0ull) break;
      const u64 m0 = __builtin_amdgcn_ballot_w64(d0 != 0u), m1 = __builtin_amdgcn_ballot_w64(d1 != 0u);
      const u64 m2 = __builtin_amdgcn_ballot_w64(d2 != 0u), m3 = __builtin_amdgcn_ballot_w64(d3 != 0u);
      if (++sp > 16) SLEEP(8);
      asm volatile("s_mov_b64 exec, %5\n\tbuffer_load_dwordx4 %0, %4, %9, 0 offen sc1\n\t"
                   "s_mov_b64 exec, %6\n\tbuffer_load_dwordx4 %1, %4, %9, 0 offen offset:1024 sc1\n\t"
                   "s_mov_b64 exec, %7\n\tbuffer_load_dwordx4 %2, %4, %9, 0 offen offset:2048 sc1\n\t"
                   "s_mov_b64 exec, %8\n\tbuffer_load_dwordx4 %3, %4, %9, 0 offen offset:3072 sc1\n\t"
                   "s_mov_b64 exec, -1"
                   : "+v"(p0), "+v"(p1), "+v"(p2), "+v"(p3) : "v"(voff), "s"(m0), "s"(m1), "s"(m2), "s"(m3), "s"(srd) : "memory");
    }
    asm volatile("s_waitcnt vmcnt(0)" : "+v"(p0), "+v"(p1), "+v"(p2), "+v"(p3) :: "memory");
  };
  {
    int po = WS_PO + ((lane >> 3) * 16 + (rg >> 1)) * 4096 + ((rg & 1) * 128 + 32 * w) * 4 + (lane & 7) * 16;
    const int so = WS_ST + (rg >> 1) * 512 + (lane & 31) * 16;
    v4u sc;
    asm volatile("buffer_load_dwordx4 %0, %2, %3, 0 offen sc1" : "=&v"(sc), "+v"(po) : "v"(so), "s"(srd) : "memory");
    { const unsigned tg = now() + GOWN; if ((int)(tg - tgate) > 0) tgate = tg; }
    SPIN sp < 32 && (int)(now() - tgate) < 0; ++sp) SLEEP(1);
    gather_issue(po);
    asm volatile("s_waitcnt vmcnt(4)" : "+v"(sc) :: "memory");
    WAIT(!__all(sc.y == tag && sc.w == tag), 16) {
      SLEEP(8);
      asm volatile("buffer_load_dwordx4 %0, %1, %2, 0 offen sc1\n\ts_waitcnt vmcnt(0)" : "=&v"(sc) : "v"(so), "s"(srd) : "memory");
    }
    f32x4 wya, wyb;
    {
      const float ms = __uint_as_float(sc.x), ls = __uint_as_float(sc.z);
      float M = fmaxf(ms, dpp<XOR1>(ms)); M = fmaxf(M, dpp<XOR2>(M));
      const float e = __builtin_amdgcn_exp2f(ms - M) * ls;
      float Lt = e + dpp<XOR1>(e); Lt += dpp<XOR2>(Lt);
      LAS unsigned char* qb = L + Q_OFF + w * 128;
      *(LAS float*)(qb + (lane & 3) * 32 + ((lane >> 2) & 7) * 4) = e / Lt;
      LDSW();
      wya = *(const LAS f32x4*)(qb + g * 16); wyb = *(const LAS f32x4*)(qb + 64 + g * 16);
    }
    WAIT(RFL(ctl[11]) == 0u, 1) { SLEEP(1); }
    FENCE();
    v4u wa[4];
    {
      LAS unsigned char* wb = L + S_OFF + (8 * g + qq) * 128 + w * 32 + pp * 8;
#pragma unroll
      for (int k = 0; k < 4; ++k) { const v2u lo = trread(wb + k * 4096), hi = trread(wb + k * 4096 + 512); wa[k] = v4u{lo.x, lo.y, hi.x, hi.y}; }
    }
    int yo = WS_Y2 + ((((g & 1) * 4 + (g >> 1)) * 7 + lc) * 4 + w) * 4096 + rg * 128 + r16 * 8, yo2 = yo + 2 * 7 * 4 * 4096;
    LAS unsigned char* xb = L + X_OFF + w * 2048 + lane * 8;
    LAS unsigned char* xr = L + X_OFF + r16 * 64 + g * 16;
    asm volatile("" : "+v"(yo), "+v"(yo2), "+v"(xb), "+v"(xr));
    gather_wait(po);
    *(LAS v2u*)(xb) = v2u{p0.x, p0.z}; *(LAS v2u*)(xb + 512) = v2u{p1.x, p1.z};
    *(LAS v2u*)(xb + 1024) = v2u{p2.x, p2.z}; *(LAS v2u*)(xb + 1536) = v2u{p3.x, p3.z};
    LDSW();
    __builtin_amdgcn_s_barrier();
    FENCE();
    v4u oa[8];
#pragma unroll
    for (int k = 0; k < 8; ++k) oa[k] = *(const LAS v4u*)(xr + k * 1024);
    __builtin_amdgcn_sched_barrier(0);
    f32x4 ya = {0.f, 0.f, 0.f, 0.f}, yb = {0.f, 0.f, 0.f, 0.f};
#pragma unroll
    for (int k = 0; k < 4; ++k) { ya = mfma(oa[2 * k], wa[k], ya); yb = mfma(oa[2 * k + 1], wa[k], yb); }
    ya = ya * wya + yb * wyb;
    auto t0 = __builtin_amdgcn_permlane32_swap(__float_as_uint(ya[0]), __float_as_uint(ya[1]), false, false);
    auto t1 = __builtin_amdgcn_permlane32_swap(__float_as_uint(ya[2]), __float_as_uint(ya[3]), false, false);
    const float v0 = __uint_as_float(t0[0]) + __uint_as_float(t0[1]), v1 = __uint_as_float(t1[0]) + __uint_as_float(t1[1]);
    __builtin_amdgcn_raw_buffer_store_b64(v2u{__float_as_uint(v0), tag}, rs, yo, 0, 16);
    __builtin_amdgcn_raw_buffer_store_b64(v2u{__float_as_uint(v1), tag}, rs, yo2, 0, 16);
  }
  if (w != 0) return;
  {
    const int nt = (bid >> 3) & 3, b3 = bid & 7;
    const int yo = WS_Y2 + ((b3 * 7 + lc) * 4 + nt) * 4096 + lane * 16;
    const __amdgpu_buffer_rsrc_t ro = __builtin_amdgcn_make_buffer_rsrc((void*)out, 0, B * D * 2, 0x00020000);
    int oo = lane < 8 ? (b3 * D + lc * 64 + nt * 16 + 2 * lane) * 2 : B * D * 2;
    asm volatile("" : "+v"(oo));
    SLEEP(D2);
    gather_issue(yo);
    gather_wait(yo);
    float v0 = (__uint_as_float(p0.x) + __uint_as_float(p1.x)) + (__uint_as_float(p2.x) + __uint_as_float(p3.x));
    float v1 = (__uint_as_float(p0.z) + __uint_as_float(p1.z)) + (__uint_as_float(p2.z) + __uint_as_float(p3.z));
    v0 += dpp<XOR8>(v0); v1 += dpp<XOR8>(v1);
    v0 = x16sum(v0); v1 = x16sum(v1);
    __builtin_amdgcn_raw_buffer_store_b32(cvtpk(v0, v1), ro, oo, 0, 0);
    __builtin_amdgcn_raw_buffer_store_b32(eraw + 1u, rs, bid == 0 ? eoff : WS_BYTES, 0, 16);
  }
}

extern "C" void kernel_launch(void* const* d_in, const int* in_sizes, int n_in, void* d_out, int out_size, void* d_ws, size_t ws_size, hipStream_t stream) {
  (void)hipFuncSetAttribute((const void*)k_fused, hipFuncAttributeMaxDynamicSharedMemorySize, LDS_BYTES);
  k_fused<<<256, NWAVE * 64, LDS_BYTES, stream>>>((const unsigned short*)d_in[0], (const unsigned short*)d_in[1], (const unsigned short*)d_in[2],
      (const unsigned short*)d_in[3], (const unsigned short*)d_in[4], (unsigned short*)d_out, (unsigned char*)d_ws);
}
```
